```python
import torch

def _softmax(x, axis=-1):
    return torch.softmax(x, dim=axis)
def _gelu(x):
    return torch.nn.functional.gelu(x, approximate="tanh")
def _silu(x):
    return torch.nn.functional.silu(x)
def _sigmoid(x):
    return torch.sigmoid(x)
def _layernorm(x, axis=-1, eps=1e-5):
    m = x.mean(dim=axis, keepdim=True)
    v = x.var(dim=axis, keepdim=True, unbiased=False)
    return (x - m) / torch.sqrt(v + eps)
def _scatter_add(tgt, idx, src, axis=0):
    return tgt.clone().index_add_(axis, idx, src)
def _segment_sum(src, seg, n):
    out = torch.zeros((n,) + src.shape[1:], dtype=src.dtype)
    return out.index_add_(0, seg, src)
def _pad(x, pw):
    flat = tuple(v for pair in reversed(pw) for v in pair)
    return torch.nn.functional.pad(x, flat)

kernel_name = 'interleave_layers__sliding_block__dense_activation_squared_relu__prompt'

def setup_inputs():

    _w = (lambda a: (lambda t: t.to(torch.bfloat16) if t.is_floating_point() else t)(torch.as_tensor(a)))
    def _A_setup():
        B, S, D = 1, 2048, 640
        return {
            "x": _w((lambda *s: torch.randn(*s, dtype=torch.bfloat16) * 0.05)(B, S, D)),
            "wqkv": _w((lambda *s: torch.randn(*s, dtype=torch.bfloat16) * 0.05)(D, 3 * D)),
            "wo": _w((lambda *s: torch.randn(*s, dtype=torch.bfloat16) * 0.05)(D, D)),
        }
    _A_in = _A_setup()

    def _B_setup():
        B, S, D, F = 1, 2048, 640, 1536
        return {
            "x": _w((lambda *s: torch.randn(*s, dtype=torch.bfloat16) * 0.05)(B, S, D)),
            "w1": _w((lambda *s: torch.randn(*s, dtype=torch.bfloat16) * 0.05)(D, F)),
            "b1": _w((lambda *s: torch.randn(*s, dtype=torch.bfloat16) * 0.2)(F)),
            "w2": _w((lambda *s: torch.randn(*s, dtype=torch.bfloat16) * 0.05)(F, D)),
            "b2": _w((lambda *s: torch.randn(*s, dtype=torch.bfloat16) * 0.8)(D)),
        }
    _B_in = _B_setup()

    return {
        "A_x": _A_in["x"],
        "A_wqkv": _A_in["wqkv"],
        "A_wo": _A_in["wo"],
        "B_x": _B_in["x"],
        "B_w1": _B_in["w1"],
        "B_b1": _B_in["b1"],
        "B_w2": _B_in["w2"],
        "B_b2": _B_in["b2"],
    }

def reference(A_x, A_wqkv, A_wo, B_x, B_w1, B_b1, B_w2, B_b2):
    def _A_ref(x, wqkv, wo):
        B, S, D = x.shape
        H = 32; Dh = D // H; W = 256
        qkv = (lambda x, *d: x.permute(*d))((x @ wqkv).reshape(B, S, 3, H, Dh), 2, 0, 3, 1, 4)
        q, k, v = qkv[0], qkv[1], qkv[2]
        scores = torch.einsum("bhqd,bhkd->bhqk", q, k) * (Dh ** -0.5)
        idx = torch.arange(S)
        mask = (idx[:, None] - idx[None, :] >= 0) & (idx[:, None] - idx[None, :] < W)
        scores = torch.where(mask[None, None], scores, -1e9)
        p = _softmax(scores, axis=-1)
        o = torch.einsum("bhqk,bhkd->bhqd", p, v)
        return (lambda x, *d: x.permute(*d))(o, 0, 2, 1, 3).reshape(B, S, D) @ wo

    def _B_ref(x, w1, b1, w2, b2):
        h = torch.einsum("bsd,df->bsf", x, w1) + b1
        zero = torch.zeros_like(h)
        r = torch.where(h > zero, h, zero)
        h = r * r
        out = torch.einsum("bsf,fd->bsd", h, w2) + b2
        return out

    x = A_x
    x = x + _A_ref(_layernorm(x, axis=-1), A_wqkv, A_wo)
    x = x + _B_ref(_layernorm(x, axis=-1), B_w1, B_b1, B_w2, B_b2)
    x = x + _A_ref(_layernorm(x, axis=-1), A_wqkv, A_wo)
    x = x + _B_ref(_layernorm(x, axis=-1), B_w1, B_b1, B_w2, B_b2)
    return x
```

```cpp
#include <hip/hip_runtime.h>
#include <hip/hip_bf16.h>
#include <stdint.h>

#define DI __device__ __forceinline__
#define LAS __attribute__((address_space(3)))
#define GAS __attribute__((address_space(1)))

typedef __bf16 bf16x8 __attribute__((ext_vector_type(8)));
typedef __bf16 bf16x4 __attribute__((ext_vector_type(4)));
typedef __bf16 bf16x2 __attribute__((ext_vector_type(2)));
typedef float f32x4 __attribute__((ext_vector_type(4)));
typedef float f32x2 __attribute__((ext_vector_type(2)));
typedef unsigned u32x4 __attribute__((ext_vector_type(4)));
typedef unsigned u32x2 __attribute__((ext_vector_type(2)));
typedef unsigned long long u64;
typedef __amdgpu_buffer_rsrc_t rsrc_t;

constexpr unsigned WS_Z = 0;
constexpr unsigned WS_TMO = 256;
constexpr unsigned WS_ARR = 4096;
constexpr unsigned WS_FLG = 8192;
constexpr unsigned WS_TS = 32768;
constexpr unsigned WS_KV = 131072;
constexpr unsigned KV_WG = 22528;
constexpr unsigned KV_L = 16 * 16 * KV_WG;
constexpr unsigned XF_L = 128 * 20 * 1024;
constexpr unsigned HF_L = 128 * 48 * 1024;
constexpr unsigned ST_L = 2048 * 64;
constexpr unsigned WS_O = WS_KV + 2 * KV_L;
constexpr unsigned WS_X1 = WS_O + 2 * XF_L;
constexpr unsigned WS_X2 = WS_X1 + 2 * XF_L;
constexpr unsigned WS_H = WS_X2 + XF_L;
constexpr unsigned WS_ST1 = WS_H + 2 * HF_L;
constexpr unsigned WS_ST2 = WS_ST1 + 2 * ST_L;
constexpr unsigned WS_END = WS_ST2 + ST_L;

constexpr int F_KV = 0, F_O = 2, F_X1 = 4, F_H = 6, F_X2 = 8;

constexpr unsigned LDS_BYTES = 163840;
constexpr unsigned L_MISC = 153600;
constexpr unsigned L_CSQ = L_MISC;
constexpr unsigned L_CS1 = L_MISC + 512;
constexpr unsigned L_KV = 0;
constexpr unsigned L_RED = 122880;

struct Params {
  const void* x; const void* wqkv; const void* wo; const void* w1; const void* b1;
  const void* w2; const void* b2; void* out; void* ws;
};

DI rsrc_t mk_rsrc(const void* p, unsigned bytes) { return __builtin_amdgcn_make_buffer_rsrc((void*)p, 0, bytes, 0x00020000); }
DI void dma16(rsrc_t rs, unsigned lds_addr, unsigned voff, unsigned soff) {
  asm volatile("s_mov_b32 m0, %0\n\ts_nop 3\n\tbuffer_load_dwordx4 %1, %2, %3 offen lds"
               :: "s"(lds_addr), "v"(voff), "s"(rs), "s"(soff) : "memory");
}
DI void wg_barrier() { asm volatile("s_waitcnt lgkmcnt(0)\n\ts_barrier" ::: "memory"); }
DI void wait_vm0() { asm volatile("s_waitcnt vmcnt(0)" ::: "memory"); }
DI u32x4 ld16(rsrc_t rs, unsigned voff, unsigned soff) { return __builtin_amdgcn_raw_buffer_load_b128(rs, voff, soff, 16); }
DI u32x2 ld8(rsrc_t rs, unsigned voff, unsigned soff) { return __builtin_amdgcn_raw_buffer_load_b64(rs, voff, soff, 16); }
DI void st16(u32x4 v, rsrc_t rs, unsigned voff, unsigned soff) { __builtin_amdgcn_raw_buffer_store_b128(v, rs, voff + soff, 0, 16); }
DI void st8(u32x2 v, rsrc_t rs, unsigned voff, unsigned soff) { __builtin_amdgcn_raw_buffer_store_b64(v, rs, voff, soff, 16); }
DI bf16x4 tr_read(LAS char* p) { return __builtin_amdgcn_ds_read_tr16_b64_v4bf16((LAS bf16x4*)p); }
DI bf16x8 cat(bf16x4 a, bf16x4 b) { return __builtin_shufflevector(a, b, 0, 1, 2, 3, 4, 5, 6, 7); }
DI f32x4 mfma(bf16x8 a, bf16x8 b, f32x4 c) { return __builtin_amdgcn_mfma_f32_16x16x32_bf16(a, b, c, 0, 0, 0); }
DI bf16x8 asbf(u32x4 v) { return __builtin_bit_cast(bf16x8, v); }
DI unsigned pk2(float a, float b) { f32x2 v = {a, b}; return __builtin_bit_cast(unsigned, __builtin_convertvector(v, bf16x2)); }
DI u32x2 pk4(f32x4 v) { u32x2 r = {pk2(v[0], v[1]), pk2(v[2], v[3])}; return r; }
DI u32x4 cat2(u32x2 a, u32x2 b) { u32x4 r = {a[0], a[1], b[0], b[1]}; return r; }
DI float bflo(unsigned u) { return __builtin_bit_cast(float, u << 16); }
DI float bfhi(unsigned u) { return __builtin_bit_cast(float, u & 0xffff0000u); }
DI f32x4 unpk4(u32x2 v) { f32x4 r = {bflo(v[0]), bfhi(v[0]), bflo(v[1]), bfhi(v[1])}; return r; }
DI float xsum(float v) {
  unsigned u = __builtin_bit_cast(unsigned, v);
  auto a = __builtin_amdgcn_permlane16_swap(u, u, false, false);
  float s = __builtin_bit_cast(float, (unsigned)a[0]) + __builtin_bit_cast(float, (unsigned)a[1]);
  unsigned w = __builtin_bit_cast(unsigned, s);
  auto b = __builtin_amdgcn_permlane32_swap(w, w, false, false);
  return __builtin_bit_cast(float, (unsigned)b[0]) + __builtin_bit_cast(float, (unsigned)b[1]);
}
DI float xmax(float v) {
  unsigned u = __builtin_bit_cast(unsigned, v);
  auto a = __builtin_amdgcn_permlane16_swap(u, u, false, false);
  float s = fmaxf(__builtin_bit_cast(float, (unsigned)a[0]), __builtin_bit_cast(float, (unsigned)a[1]));
  unsigned w = __builtin_bit_cast(unsigned, s);
  auto b = __builtin_amdgcn_permlane32_swap(w, w, false, false);
  return fmaxf(__builtin_bit_cast(float, (unsigned)b[0]), __builtin_bit_cast(float, (unsigned)b[1]));
}
DI float bcast_row1(float v) {
  unsigned u = __builtin_bit_cast(unsigned, v);
  auto a = __builtin_amdgcn_permlane16_swap(u, u, false, false);
  unsigned w = a[1];
  auto b = __builtin_amdgcn_permlane32_swap(w, w, false, false);
  return __builtin_bit_cast(float, (unsigned)b[0]);
}

struct Ctx {
  LAS char* lds; unsigned lds0;
  rsrc_t rws;
  GAS u64* flg;
  u64 tag;
  int rb, j, w, lane, m, g;
  unsigned la0, la1, ls, lh;
  bool dead;
  GAS unsigned* tmo;
};

DI void refresh(Ctx& c) {
  int l = c.lane, w = c.w, j = c.j, rb = c.rb; unsigned l0 = c.lds0;
  asm volatile("" : "+v"(l), "+s"(w), "+s"(j), "+s"(rb), "+s"(l0));
  c.lane = l; c.w = w; c.j = j; c.rb = rb; c.lds0 = l0;
  c.lds = (LAS char*)(size_t)l0;
  c.m = l & 15; c.g = l >> 4;
  const int g = c.g, q = (l >> 2) & 3, pp = l & 3;
  c.la0 = (4 * g + q) * 64 + (2 * (g & 1) + (pp >> 1)) * 16 + (pp & 1) * 8;
  c.la1 = (4 * g + q) * 64 + (2 * (1 ^ (g & 1)) + (pp >> 1)) * 16 + (pp & 1) * 8;
  c.ls = (4 * g + q) * 32 + pp * 8;
  c.lh = (4 * g + q) * 16 + (pp & 1) * 8;
}
#define PHASE_VARS refresh(c); const int rb = c.rb, j = c.j, w = c.w, lane = c.lane, m = c.m, g = c.g; \
  const int hf = j & 1, cs8 = j >> 1, rg = w & 1, kh = w >> 1; const int mtE = 8 * rb + 4 * hf + 2 * rg + kh; \
  (void)hf; (void)cs8; (void)rg; (void)kh; (void)mtE; (void)lane; (void)m; (void)g;

DI void wait_flags(Ctx& c, int phase, int rb, int first, int n) {
  GAS u64* p = c.flg + (phase * 16 + rb) * 16 + first + (c.lane < n ? c.lane : 0);
  unsigned spins = 0;
  while (!c.dead) {
    u64 v = __hip_atomic_load(p, __ATOMIC_RELAXED, __HIP_MEMORY_SCOPE_AGENT);
    if (__all(v == c.tag)) break;
    if (++spins > 40000u) { c.dead = true; if (c.lane == 0) __hip_atomic_store(c.tmo, (unsigned)(phase + 1), __ATOMIC_RELAXED, __HIP_MEMORY_SCOPE_AGENT); }
    __builtin_amdgcn_s_sleep(1);
  }
  asm volatile("" ::: "memory");
}
DI void set_flag(Ctx& c, int phase) {
  if (threadIdx.x == 0) __hip_atomic_store(c.flg + (phase * 16 + c.rb) * 16 + c.j, c.tag, __ATOMIC_RELAXED, __HIP_MEMORY_SCOPE_AGENT);
}

DI unsigned qkv_col(int cc, int j) { return (unsigned)(cc < 5 ? 40 * j + 8 * cc : (cc < 10 ? 640 + 40 * j + 8 * (cc - 5) : 1280 + 40 * j + 8 * (cc - 10))); }

DI void stage_qkv(const Ctx& c, rsrc_t rs, bool first) {
  const int l = c.lane, r = l >> 2, ch = (l & 3) ^ (2 * ((l >> 4) & 1));
  const unsigned ro = first ? 8 * (r >> 2) + (r & 3) : r;
  unsigned vp[3];
#pragma unroll
  for (int u = 0; u < 3; ++u) vp[u] = ro * 3840u + qkv_col(4 * u + ch, c.j) * 2u;
  const unsigned tadv = (first ? 4u : 16u) * 3840u;
  const int rho = l >> 1, r2 = rho & 15;
  const unsigned srow = first ? 8 * (r2 >> 2) + 4 * (rho >> 4) + (r2 & 3) : rho;
  const unsigned vs = srow * 3840u + qkv_col(12 + (l & 1), c.j) * 2u;
  const int rh = l & 31, r3 = rh & 15;
  const unsigned hrow = 32 * (l >> 5) + (first ? 8 * (r3 >> 2) + 4 * (rh >> 4) + (r3 & 3) : rh);
  const unsigned vh = hrow * 3840u + qkv_col(14, c.j) * 2u;
#pragma unroll
  for (int it = 0; it < 5; ++it) {
    const unsigned s = c.w + 4 * it, sb = s * (32u * 3840u), ld = c.lds0 + s * 7168u;
#pragma unroll
    for (int t = 0; t < 2; ++t)
#pragma unroll
      for (int u = 0; u < 3; ++u) dma16(rs, ld + (t * 3 + u) * 1024u, vp[u], sb + t * tadv);
    dma16(rs, ld + 6144u, vs, sb);
  }
  for (unsigned i = c.w; i < 10; i += 4) dma16(rs, c.lds0 + 143360u + i * 1024u, vh, i * (64u * 3840u));
}

DI void stage_w1(const Ctx& c, rsrc_t rs) {
  const int l = c.lane, r = l >> 2, ch = (l & 3) ^ (2 * ((l >> 4) & 1));
  const unsigned vp = r * 3072u + c.j * 192u + ch * 16u;
#pragma unroll
  for (int it = 0; it < 5; ++it) {
    const unsigned s = c.w + 4 * it, sb = s * (32u * 3072u), ld = c.lds0 + s * 6144u;
#pragma unroll
    for (int t = 0; t < 2; ++t)
#pragma unroll
      for (int u = 0; u < 3; ++u) dma16(rs, ld + (t * 3 + u) * 1024u, vp, sb + t * (16u * 3072u) + u * 64u);
  }
}

DI void stage80_step(rsrc_t rs, unsigned ld, unsigned vp, unsigned vs, unsigned sb, unsigned tadv) {
  dma16(rs, ld, vp, sb);
  dma16(rs, ld + 1024u, vp, sb + 64u);
  dma16(rs, ld + 2048u, vp, sb + tadv);
  dma16(rs, ld + 3072u, vp, sb + tadv + 64u);
  dma16(rs, ld + 4096u, vs, sb);
}
DI void stage_wo(const Ctx& c, rsrc_t rs) {
  const int l = c.lane, r = l >> 2, ch = (l & 3) ^ (2 * ((l >> 4) & 1));
  const unsigned cb = (c.j >> 1) * 160u;
  const int rho = l >> 1, r2 = rho & 15;
  const unsigned vpA = r * 1280u + cb + ch * 16u;
  const unsigned vsA = (20 * (rho >> 4) + r2) * 1280u + cb + 128u + (l & 1) * 16u;
  const unsigned vpB = (40 * (r >> 2) + (r & 3)) * 1280u + cb + ch * 16u;
  const unsigned vsB = (40 * (r2 >> 2) + 20 * (rho >> 4) + (r2 & 3)) * 1280u + cb + 128u + (l & 1) * 16u;
#pragma unroll
  for (int it = 0; it < 4; ++it) {
    const unsigned s = c.w + 4 * it;
    stage80_step(rs, c.lds0 + s * 5120u, vpA, vsA, s * (40u * 1280u), 20u * 1280u);
  }
  {
    const unsigned s = 16 + c.w;
    stage80_step(rs, c.lds0 + s * 5120u, vpB, vsB, (160u * c.w + 16u) * 1280u, 20u * 1280u);
  }
}
DI void w2_lane(const Ctx& c, unsigned& vp, unsigned& vs) {
  const int l = c.lane, r = l >> 2, ch = (l & 3) ^ (2 * ((l >> 4) & 1));
  const unsigned cb = (c.j >> 1) * 160u;
  vp = r * 1280u + cb + ch * 16u;
  vs = (l >> 1) * 1280u + cb + 128u + (l & 1) * 16u;
}
DI void stage_w2_first(const Ctx& c, rsrc_t rs) {
  unsigned vp, vs; w2_lane(c, vp, vs);
  for (unsigned q = c.w; q < 30; q += 4) {
    const unsigned kh = q >= 15 ? 1 : 0, s = 24 * kh + (q - 15 * kh);
    stage80_step(rs, c.lds0 + q * 5120u, vp, vs, s * (32u * 1280u), 16u * 1280u);
  }
}
DI void stage_w2_refill(const Ctx& c, rsrc_t rs) {
  unsigned vp, vs; w2_lane(c, vp, vs);
  for (unsigned e = c.w; e < 18; e += 4) {
    const unsigned kh = e >= 9 ? 1 : 0, i = e - 9 * kh;
    stage80_step(rs, c.lds0 + (15 * kh + i) * 5120u, vp, vs, (24 * kh + 15 + i) * (32u * 1280u), 16u * 1280u);
  }
}

DI bf16x8 wfrag_pair(const Ctx& c, unsigned off0, unsigned off1, int tt) {
  const unsigned la = tt ? c.la1 : c.la0;
  return cat(tr_read(c.lds + off0 + la), tr_read(c.lds + off1 + la));
}
DI bf16x8 wfrag_single(const Ctx& c, unsigned off) { return cat(tr_read(c.lds + off + c.ls), tr_read(c.lds + off + 512u + c.ls)); }
DI bf16x8 wfrag_qkv(const Ctx& c, int s, int t) {
  if (t < 6) return wfrag_pair(c, s * 7168u + (t >> 1) * 1024u, s * 7168u + (3 + (t >> 1)) * 1024u, t & 1);
  if (t == 6) return wfrag_single(c, s * 7168u + 6144u);
  return cat(tr_read(c.lds + 143360u + s * 512u + c.lh), tr_read(c.lds + 143360u + s * 512u + 256u + c.lh));
}
DI bf16x8 wfrag_w1(const Ctx& c, int s, int t) {
  return wfrag_pair(c, s * 6144u + (t >> 1) * 1024u, s * 6144u + (3 + (t >> 1)) * 1024u, t & 1);
}
DI bf16x8 wfrag_80(const Ctx& c, unsigned slot, int t) {
  if (t < 4) return wfrag_pair(c, slot + (t >> 1) * 1024u, slot + (2 + (t >> 1)) * 1024u, t & 1);
  return wfrag_single(c, slot + 4096u);
}

template <int... Is> struct iseq {};
template <int N, int... Is> struct mk_iseq : mk_iseq<N - 1, N - 1, Is...> {};
template <int... Is> struct mk_iseq<0, Is...> { typedef iseq<Is...> type; };
constexpr int PD = 4;
template <int I, int NS, int NT, int A0, int NA, typename F>
DI void gemm_group(f32x4 (&acc)[2][NT], const u32x4 (&a)[2][NA], bf16x8 (&wf)[PD + 1], F& rd) {
  constexpr int N = NS * NT, s = I / NT, t = I % NT;
  if constexpr (I + PD < N) wf[(I + PD) % (PD + 1)] = rd((I + PD) / NT, (I + PD) % NT);
  acc[0][t] = mfma(wf[I % (PD + 1)], asbf(a[0][A0 + s]), acc[0][t]);
  acc[1][t] = mfma(wf[I % (PD + 1)], asbf(a[1][A0 + s]), acc[1][t]);
  __builtin_amdgcn_sched_barrier(0);
}
template <int NS, int NT, int A0, int NA, typename F, int... Is>
DI void gemm_steps_(f32x4 (&acc)[2][NT], const u32x4 (&a)[2][NA], F& rd, iseq<Is...>) {
  bf16x8 wf[PD + 1];
  wf[0] = rd(0 / NT, 0 % NT); wf[1] = rd(1 / NT, 1 % NT); wf[2] = rd(2 / NT, 2 % NT); wf[3] = rd(3 / NT, 3 % NT);
  (gemm_group<Is, NS, NT, A0, NA>(acc, a, wf, rd), ...);
}
template <int NS, int NT, int A0, int NA, typename F>
DI void gemm_steps(f32x4 (&acc)[2][NT], const u32x4 (&a)[2][NA], F rd) {
  gemm_steps_<NS, NT, A0, NA>(acc, a, rd, typename mk_iseq<NS * NT>::type());
}

DI void row_stats(float s, float q, float& mean, float& rstd) {
  mean = s * (1.0f / 640.0f);
  const float var = fmaxf(q * (1.0f / 640.0f) - mean * mean, 0.0f);
  rstd = __builtin_amdgcn_rsqf(var + 1e-5f);
}

template <bool EDGE>
DI void attn_head(const Ctx& c, int h, int first_valid, const bf16x8 (&qf)[2], u32x2 (&omain)[2], u32x2 (&oleft)[2]) {
  const int g = c.g, m = c.m, l = c.lane;
  LAS char* base = c.lds + L_KV + (2 * c.w) * 2816 + h * 1408;
  const int qa = h == 0 ? g : (g > 0 ? g - 1 : 0), qb = h == 0 ? 4 : (g < 1 ? 3 : 4);
  LAS char* ka = base + qa * 128 + m * 8;
  LAS char* kb = base + qb * 128 + m * 8;
  const int q4 = (l >> 2) & 3, p = l & 3;
  LAS char* v0 = base + 640 + (g >> 1) * 384 + p * 64 + (4 * (g & 1) + q4) * 8;
  LAS char* v1 = base + 640 + (g >> 1) * 384 + (4 + (p & 1)) * 64 + (4 * (g & 1) + q4) * 8;
  const float NEG = -__builtin_inff();
  f32x4 S[2][17];
  const f32x4 z4 = {0.f, 0.f, 0.f, 0.f};
#pragma unroll
  for (int i = 0; i < 18; ++i) {
    const bool ok = !EDGE || (2 * c.w + i >= first_valid);
    if (ok) {
      const bf16x4 lo = *(LAS bf16x4*)(ka + i * 2816), hi = *(LAS bf16x4*)(kb + i * 2816);
      const bf16x8 kf = cat(lo, hi);
      if (i <= 16) S[0][i] = mfma(kf, qf[0], z4);
      if (i >= 1) S[1][i - 1] = mfma(kf, qf[1], z4);
    } else {
      const f32x4 n4 = {NEG, NEG, NEG, NEG};
      if (i <= 16) S[0][i] = n4;
      if (i >= 1) S[1][i - 1] = n4;
    }
  }
  const float cs = 0.22360679774997896f * 1.4426950408889634f;
  bf16x8 P[2][9];
#pragma unroll
  for (int i = 0; i < 2; ++i) {
#pragma unroll
    for (int r = 0; r < 4; ++r) {
      S[i][0][r] = (4 * g + r > m) ? S[i][0][r] : NEG;
      S[i][16][r] = (4 * g + r <= m) ? S[i][16][r] : NEG;
    }
    float mx = fmaxf(fmaxf(S[i][0][0], S[i][0][1]), fmaxf(S[i][0][2], S[i][0][3]));
#pragma unroll
    for (int t = 1; t < 17; ++t) mx = fmaxf(fmaxf(fmaxf(mx, S[i][t][0]), S[i][t][1]), fmaxf(S[i][t][2], S[i][t][3]));
    mx = xmax(mx);
    const float mc = -mx * cs;
    unsigned pk[18][2];
#pragma unroll
    for (int t = 0; t < 17; ++t) {
      float e[4];
#pragma unroll
      for (int r = 0; r < 4; ++r) e[r] = __builtin_amdgcn_exp2f(__builtin_fmaf(S[i][t][r], cs, mc));
      pk[t + i][0] = pk2(e[0], e[1]); pk[t + i][1] = pk2(e[2], e[3]);
    }
    pk[i == 0 ? 17 : 0][0] = 0u; pk[i == 0 ? 17 : 0][1] = 0u;
#pragma unroll
    for (int st = 0; st < 9; ++st) { u32x4 v = {pk[2 * st][0], pk[2 * st][1], pk[2 * st + 1][0], pk[2 * st + 1][1]}; P[i][st] = asbf(v); }
  }
  f32x4 O[2][2] = {{z4, z4}, {z4, z4}};
#pragma unroll
  for (int st = 0; st < 9; ++st) {
    const bool ok = !EDGE || (2 * c.w + 2 * st >= first_valid);
    if (ok) {
      const bf16x8 vf0 = cat(tr_read(v0 + (2 * st) * 2816), tr_read(v0 + (2 * st + 1) * 2816));
      const bf16x8 vf1 = cat(tr_read(v1 + (2 * st) * 2816), tr_read(v1 + (2 * st + 1) * 2816));
#pragma unroll
      for (int i = 0; i < 2; ++i) { O[i][0] = mfma(vf0, P[i][st], O[i][0]); O[i][1] = mfma(vf1, P[i][st], O[i][1]); }
    }
  }
#pragma unroll
  for (int i = 0; i < 2; ++i) {
    const float inv = __builtin_amdgcn_rcpf(bcast_row1(O[i][1][0]));
    omain[i] = pk4(O[i][0] * inv);
    oleft[i] = pk4(O[i][1] * inv);
  }
}

#define TS(k) do {} while (0)

__global__ void __launch_bounds__(256, 1) fused_kernel(Params p) {
  extern __shared__ __attribute__((aligned(16))) char smem[];
  Ctx c;
  c.lds = (LAS char*)smem;
  c.lds0 = (unsigned)(size_t)c.lds;
  const int b = blockIdx.x, xcd = b & 7, loc = b >> 3;
  c.rb = 2 * xcd + (loc >> 4); c.j = loc & 15;
  c.lane = threadIdx.x & 63;
  c.w = __builtin_amdgcn_readfirstlane(threadIdx.x >> 6);
  c.rws = mk_rsrc(p.ws, WS_END);
  c.flg = (GAS u64*)((char*)p.ws + WS_FLG);
  c.tmo = (GAS unsigned*)((char*)p.ws + WS_TMO);
  c.dead = false;
  const rsrc_t rx = mk_rsrc(p.x, 2048u * 640u * 2u);
  const rsrc_t rwqkv = mk_rsrc(p.wqkv, 640u * 1920u * 2u), rwo = mk_rsrc(p.wo, 640u * 640u * 2u);
  const rsrc_t rw1 = mk_rsrc(p.w1, 640u * 1536u * 2u), rw2 = mk_rsrc(p.w2, 1536u * 640u * 2u);
  const rsrc_t rb1 = mk_rsrc(p.b1, 1536u * 2u), rb2 = mk_rsrc(p.b2, 640u * 2u), rout = mk_rsrc(p.out, 2048u * 640u * 2u);
  TS(0);

  u64 zval = *(const __attribute__((address_space(4))) u64*)((const char*)p.ws + WS_Z);
  asm volatile("" : "+s"(zval));
  u32x2 xres[5];
  {
    PHASE_VARS
    stage_qkv(c, rwqkv, true);
    const unsigned vo = (mtE * 16 + m) * 1280u + cs8 * 160u + g * 8u;
#pragma unroll
    for (int t = 0; t < 5; ++t) xres[t] = ld8(rx, vo, t * 32u);
  }
  c.tag = zval * 0x9E3779B97F4A7C15ull + 0x632BE59BD9B4E019ull;
  const f32x4 z4 = {0.f, 0.f, 0.f, 0.f};
  const u32x4 ones_u = {0x3f803f80u, 0x3f803f80u, 0x3f803f80u, 0x3f803f80u};

#pragma unroll 1
  for (int L = 0; L < 2; ++L) {
    {
      PHASE_VARS
      u32x4 a[2][20];
      float mean[2], rstd[2];
      u32x4 st[2];
      if (L == 1) wait_flags(c, F_X2, rb, 0, 16);
      {
#pragma unroll
        for (int i = 0; i < 2; ++i) {
          const unsigned mt = 8 * rb + 2 * w + i;
          if (L == 0) {
#pragma unroll
            for (int s = 0; s < 20; ++s) a[i][s] = ld16(rx, m * 1280u + g * 16u + s * 64u, mt * (16u * 1280u));
          } else {
#pragma unroll
            for (int s = 0; s < 20; ++s) a[i][s] = ld16(c.rws, lane * 16u + (s & 3) * 1024u, WS_X2 + mt * 20480u + (s >> 2) * 4096u);
          }
          if (L == 1) st[i] = ld16(c.rws, m * 64u + g * 16u, WS_ST2 + mt * 1024u);
        }
      }
      __builtin_amdgcn_sched_barrier(0);
      asm volatile("s_waitcnt vmcnt(40)" ::: "memory");
      wg_barrier();
      TS(1 + 12 * L);
      if (L == 0) {
#pragma unroll
        for (int e = 0; e < 2; ++e) {
          const int t = w + 4 * e;
          const unsigned la = (t & 1) ? c.la1 : c.la0;
          const unsigned o0 = t < 6 ? (t >> 1) * 1024u + la : (t == 6 ? 6144u + c.ls : 143360u + c.lh);
          const unsigned ot = t < 6 ? 3072u : (t == 6 ? 512u : 256u), os = t < 7 ? 7168u : 512u;
          f32x4 acc = z4;
#pragma unroll
          for (int s = 0; s < 20; ++s) acc = mfma(cat(tr_read(c.lds + o0 + s * os), tr_read(c.lds + o0 + ot + s * os)), asbf(ones_u), acc);
          if (m == 0) *(LAS f32x4*)(c.lds + L_CSQ + (16 * t + 4 * g) * 4) = acc;
        }
      }
      f32x4 acc[2][8];
#pragma unroll
      for (int t = 0; t < 8; ++t) { acc[0][t] = z4; acc[1][t] = z4; }
      gemm_steps<20, 8, 0, 20>(acc, a, [&](int s, int t) __attribute__((always_inline)) { return wfrag_qkv(c, s, t); });
      if (L == 0) {
#pragma unroll
        for (int i = 0; i < 2; ++i) {
          float s0 = 0.f, q0 = 0.f, s1 = 0.f, q1 = 0.f;
          const bf16x2 one2 = {(__bf16)1.0f, (__bf16)1.0f};
#pragma unroll
          for (int s = 0; s < 20; ++s)
#pragma unroll
            for (int e = 0; e < 4; e += 2) {
              const unsigned u0 = a[i][s][e], u1 = a[i][s][e + 1];
              const bf16x2 x0 = __builtin_bit_cast(bf16x2, u0), x1 = __builtin_bit_cast(bf16x2, u1);
              s0 = __builtin_amdgcn_fdot2_f32_bf16(x0, one2, s0, false); q0 = __builtin_amdgcn_fdot2_f32_bf16(x0, x0, q0, false);
              s1 = __builtin_amdgcn_fdot2_f32_bf16(x1, one2, s1, false); q1 = __builtin_amdgcn_fdot2_f32_bf16(x1, x1, q1, false);
            }
          row_stats(xsum(s0 + s1), xsum(q0 + q1), mean[i], rstd[i]);
        }
      } else {
#pragma unroll
        for (int i = 0; i < 2; ++i) {
          const f32x4 v = __builtin_bit_cast(f32x4, st[i]);
          row_stats(xsum(v[0] + v[2]), xsum(v[1] + v[3]), mean[i], rstd[i]);
        }
      }
      TS(2 + 12 * L);
      wg_barrier();
      if (L == 0 && threadIdx.x == 0)
        __hip_atomic_store((GAS u64*)((char*)p.ws + WS_ARR) + b, c.tag, __ATOMIC_RELAXED, __HIP_MEMORY_SCOPE_AGENT);
      bf16x8 qf[2][2];
      {
        const unsigned vb = 640u + (m >> 3) * 384u + (m & 7) * 8u;
        unsigned ko[6];
        ko[0] = (unsigned)(g - 2) * 128u + m * 8u;
        ko[1] = g < 3 ? (2 + g) * 128u + m * 8u : 1408u + m * 8u;
        ko[2] = 1408u + (1 + g) * 128u + m * 8u;
        ko[3] = vb + g * 64u;
        ko[4] = g == 0 ? vb + 256u : 1408u + vb + (g - 1) * 64u;
        ko[5] = g < 2 ? 1408u + vb + (3 + g) * 64u : (g == 2 ? vb + 320u : 1408u + vb + 320u);
        const unsigned kvg = WS_KV + L * KV_L + (rb * 16 + j) * KV_WG;
#pragma unroll
        for (int i = 0; i < 2; ++i) {
          u32x2 pk[8];
#pragma unroll
          for (int t = 0; t < 8; ++t) {
            const f32x4 cs = *(LAS f32x4*)(c.lds + L_CSQ + (16 * t + 4 * g) * 4);
            pk[t] = pk4((acc[i][t] - mean[i] * cs) * rstd[i]);
          }
          const u32x2 zz = {0u, 0u};
          qf[0][i] = asbf(cat2(pk[0], g == 0 ? pk[1] : zz));
          qf[1][i] = asbf(cat2(g == 0 ? zz : pk[1], g < 2 ? pk[2] : zz));
          if (g >= 2) { pk[7][0] = 0x00003f80u; pk[7][1] = 0u; }
          const unsigned tl = 2 * w + i;
          LAS char* ld = c.lds + L_KV + (16 + tl) * 2816;
#pragma unroll
          for (int t = 2; t < 8; ++t) {
            if (t > 2 || g >= 2) {
              *(LAS u32x2*)(ld + ko[t - 2]) = pk[t];
              st8(pk[t], c.rws, ko[t - 2], kvg + tl * 2816u);
            }
          }
        }
      }
      wait_vm0();
      wg_barrier();
      set_flag(c, F_KV + L);
      TS(3 + 12 * L);
      if (rb >= 1) {
        if (rb >= 2) wait_flags(c, F_KV + L, rb - 2, j, 1);
        wait_flags(c, F_KV + L, rb - 1, j, 1);
        u32x4 hv[2][6];
        const unsigned tid16 = threadIdx.x * 16u;
#pragma unroll
        for (int e = 0; e < 2; ++e) {
          const int rsrc_rb = rb - 2 + e;
          const unsigned sb = WS_KV + L * KV_L + ((rsrc_rb < 0 ? 0 : rsrc_rb) * 16 + j) * KV_WG;
#pragma unroll
          for (int i = 0; i < 6; ++i) hv[e][i] = ld16(c.rws, (i * 4096u + tid16 < KV_WG) ? i * 4096u + tid16 : 0u, sb);
        }
#pragma unroll
        for (int e = 0; e < 2; ++e)
#pragma unroll
          for (int i = 0; i < 6; ++i)
            if (i * 4096u + tid16 < KV_WG) *(LAS u32x4*)(c.lds + L_KV + e * KV_WG + i * 4096u + tid16) = hv[e][i];
      }
      wg_barrier();
      TS(4 + 12 * L);
      u32x2 om[2][2], ol[2][2];
      if (rb >= 2) {
#pragma unroll 1
        for (int h = 0; h < 2; ++h) {
          bf16x8 q2[2] = {h ? qf[1][0] : qf[0][0], h ? qf[1][1] : qf[0][1]};
          u32x2 a0[2], a1[2];
          attn_head<false>(c, h, 0, q2, a0, a1);
          if (h == 0) { om[0][0] = a0[0]; om[0][1] = a0[1]; ol[0][0] = a1[0]; ol[0][1] = a1[1]; }
          else { om[1][0] = a0[0]; om[1][1] = a0[1]; ol[1][0] = a1[0]; ol[1][1] = a1[1]; }
        }
      } else {
#pragma unroll 1
        for (int h = 0; h < 2; ++h) {
          bf16x8 q2[2] = {h ? qf[1][0] : qf[0][0], h ? qf[1][1] : qf[0][1]};
          u32x2 a0[2], a1[2];
          attn_head<true>(c, h, 16 - 8 * rb, q2, a0, a1);
          if (h == 0) { om[0][0] = a0[0]; om[0][1] = a0[1]; ol[0][0] = a1[0]; ol[0][1] = a1[1]; }
          else { om[1][0] = a0[0]; om[1][1] = a0[1]; ol[1][0] = a1[0]; ol[1][1] = a1[1]; }
        }
      }
      TS(5 + 12 * L);
      wg_barrier();
#pragma unroll
      for (int i = 0; i < 2; ++i) {
        const unsigned mt = 8 * rb + 2 * w + i, ob = WS_O + L * XF_L + mt * 20480u;
        st16(cat2(om[0][i], om[1][i]), c.rws, lane * 16u, ob + j * 1024u);
        if (g == 0) st16(cat2(ol[0][i], ol[1][i]), c.rws, m * 16u, ob + (16 + (j >> 2)) * 1024u + (j & 3) * 256u);
      }
      stage_wo(c, rwo);
      asm volatile("s_waitcnt vmcnt(25)" ::: "memory");
      wg_barrier();
      set_flag(c, F_O + L);
    }
    {
      PHASE_VARS
      u32x4 a[2][10];
      wait_flags(c, F_O + L, rb, 0, 16);
#pragma unroll
      for (int i = 0; i < 2; ++i) {
        const unsigned mt = 8 * rb + 4 * hf + 2 * rg + i;
#pragma unroll
        for (int s = 0; s < 10; ++s) a[i][s] = ld16(c.rws, lane * 16u + (s & 3) * 1024u, WS_O + L * XF_L + mt * 20480u + (10 * kh + (s & ~3)) * 1024u);
      }
      __builtin_amdgcn_sched_barrier(0);
      asm volatile("s_waitcnt vmcnt(20)" ::: "memory");
      wg_barrier();
      TS(6 + 12 * L);
      f32x4 acc[2][5];
#pragma unroll
      for (int t = 0; t < 5; ++t) { acc[0][t] = z4; acc[1][t] = z4; }
      const unsigned sl0 = kh * 10 * 5120u;
      gemm_steps<10, 5, 0, 10>(acc, a, [&](int s, int t) __attribute__((always_inline)) { return wfrag_80(c, sl0 + s * 5120u, t); });
      LAS char* red = c.lds + L_RED;
#pragma unroll
      for (int t = 0; t < 5; ++t) *(LAS f32x4*)(red + w * 5120 + t * 1024 + lane * 16) = kh ? acc[0][t] : acc[1][t];
      wg_barrier();
      float sum = 0.f, sq = 0.f;
      u32x2 pk[5];
#pragma unroll
      for (int t = 0; t < 5; ++t) {
        const f32x4 o = *(LAS f32x4*)(red + (w ^ 2) * 5120 + t * 1024 + lane * 16);
        const f32x4 mine = kh ? acc[1][t] : acc[0][t];
        pk[t] = pk4(unpk4(xres[t]) + (mine + o));
        xres[t] = pk[t];
        const f32x4 xr = unpk4(pk[t]);
#pragma unroll
        for (int r = 0; r < 4; ++r) { sum += xr[r]; sq = __builtin_fmaf(xr[r], xr[r], sq); }
      }
      sum = xsum(sum); sq = xsum(sq);
      {
        const unsigned xb = WS_X1 + L * XF_L + mtE * 20480u + ((5 * cs8) >> 1) * 1024u;
        if ((cs8 & 1) == 0) {
          st16(cat2(pk[0], pk[1]), c.rws, lane * 16u, xb);
          st16(cat2(pk[2], pk[3]), c.rws, lane * 16u, xb + 1024u);
          st8(pk[4], c.rws, lane * 16u, xb + 2048u);
        } else {
          st8(pk[0], c.rws, lane * 16u + 8u, xb);
          st16(cat2(pk[1], pk[2]), c.rws, lane * 16u, xb + 1024u);
          st16(cat2(pk[3], pk[4]), c.rws, lane * 16u, xb + 2048u);
        }
        if (g == 0) { u32x2 v = {__builtin_bit_cast(unsigned, sum), __builtin_bit_cast(unsigned, sq)}; st8(v, c.rws, m * 64u + cs8 * 8u, WS_ST1 + L * ST_L + mtE * 1024u); }
      }
      stage_w1(c, rw1);
      asm volatile("s_waitcnt vmcnt(30)" ::: "memory");
      wg_barrier();
      set_flag(c, F_X1 + L);
      TS(7 + 12 * L);
    }
    {
      PHASE_VARS
      u32x4 a[2][20];
      u32x4 st[2];
      u32x2 bb[6];
#pragma unroll
      for (int t = 0; t < 6; ++t) bb[t] = ld8(rb1, g * 8u, j * 192u + t * 32u);
      wait_flags(c, F_X1 + L, rb, 0, 16);
#pragma unroll
      for (int i = 0; i < 2; ++i) {
        const unsigned mt = 8 * rb + 2 * w + i;
#pragma unroll
        for (int s = 0; s < 20; ++s) a[i][s] = ld16(c.rws, lane * 16u + (s & 3) * 1024u, WS_X1 + L * XF_L + mt * 20480u + (s & ~3) * 1024u);
        st[i] = ld16(c.rws, m * 64u + g * 16u, WS_ST1 + L * ST_L + mt * 1024u);
      }
      __builtin_amdgcn_sched_barrier(0);
      asm volatile("s_waitcnt vmcnt(42)" ::: "memory");
      wg_barrier();
      TS(8 + 12 * L);
      if (L == 0) {
#pragma unroll
        for (int e = 0; e < 2; ++e) {
          const int t = w + 4 * e;
          if (t < 6) {
            const unsigned o0 = (t >> 1) * 1024u + ((t & 1) ? c.la1 : c.la0);
            f32x4 acc = z4;
#pragma unroll
            for (int s = 0; s < 20; ++s) acc = mfma(cat(tr_read(c.lds + o0 + s * 6144u), tr_read(c.lds + o0 + 3072u + s * 6144u)), asbf(ones_u), acc);
            if (m == 0) *(LAS f32x4*)(c.lds + L_CS1 + (16 * t + 4 * g) * 4) = acc;
          }
        }
      }
      f32x4 acc[2][6];
#pragma unroll
      for (int t = 0; t < 6; ++t) { acc[0][t] = z4; acc[1][t] = z4; }
      gemm_steps<20, 6, 0, 20>(acc, a, [&](int s, int t) __attribute__((always_inline)) { return wfrag_w1(c, s, t); });
      wg_barrier();
#pragma unroll
      for (int i = 0; i < 2; ++i) {
        const f32x4 sv = __builtin_bit_cast(f32x4, st[i]);
        float mean, rstd;
        row_stats(xsum(sv[0] + sv[2]), xsum(sv[1] + sv[3]), mean, rstd);
        u32x2 pk[6];
#pragma unroll
        for (int t = 0; t < 6; ++t) {
          const f32x4 cs = *(LAS f32x4*)(c.lds + L_CS1 + (16 * t + 4 * g) * 4);
          f32x4 v = (acc[i][t] - mean * cs) * rstd + unpk4(bb[t]);
#pragma unroll
          for (int r = 0; r < 4; ++r) { const float x = fmaxf(v[r], 0.f); v[r] = x * x; }
          pk[t] = pk4(v);
        }
        const unsigned hb = WS_H + L * HF_L + (8 * rb + 2 * w + i) * 49152u + 3 * j * 1024u;
#pragma unroll
        for (int u = 0; u < 3; ++u) st16(cat2(pk[2 * u], pk[2 * u + 1]), c.rws, lane * 16u, hb + u * 1024u);
      }
      wait_vm0();
      stage_w2_first(c, rw2);
      wg_barrier();
      set_flag(c, F_H + L);
      TS(9 + 12 * L);
    }
    {
      PHASE_VARS
      u32x4 a[2][24];
      u32x2 bb[5];
#pragma unroll
      for (int t = 0; t < 5; ++t) bb[t] = ld8(rb2, g * 8u, cs8 * 160u + t * 32u);
      wait_flags(c, F_H + L, rb, 0, 16);
#pragma unroll
      for (int s = 0; s < 24; ++s)
#pragma unroll
        for (int i = 0; i < 2; ++i) {
          const unsigned mt = 8 * rb + 4 * hf + 2 * rg + i;
          a[i][s] = ld16(c.rws, lane * 16u + (s & 3) * 1024u, WS_H + L * HF_L + mt * 49152u + (24 * kh + (s & ~3)) * 1024u);
        }
      __builtin_amdgcn_sched_barrier(0);
      asm volatile("s_waitcnt vmcnt(48)" ::: "memory");
      wg_barrier();
      TS(10 + 12 * L);
      f32x4 acc[2][5];
#pragma unroll
      for (int t = 0; t < 5; ++t) { acc[0][t] = z4; acc[1][t] = z4; }
      const unsigned sl0 = kh * 15 * 5120u;
      gemm_steps<9, 5, 0, 24>(acc, a, [&](int s, int t) __attribute__((always_inline)) { return wfrag_80(c, sl0 + s * 5120u, t); });
      wg_barrier();
      stage_w2_refill(c, rw2);
      gemm_steps<6, 5, 9, 24>(acc, a, [&](int s, int t) __attribute__((always_inline)) { return wfrag_80(c, sl0 + (9 + s) * 5120u, t); });
      wait_vm0();
      wg_barrier();
      gemm_steps<9, 5, 15, 24>(acc, a, [&](int s, int t) __attribute__((always_inline)) { return wfrag_80(c, sl0 + s * 5120u, t); });
      wg_barrier();
      LAS char* red = c.lds + L_RED;
#pragma unroll
      for (int t = 0; t < 5; ++t) *(LAS f32x4*)(red + w * 5120 + t * 1024 + lane * 16) = kh ? acc[0][t] : acc[1][t];
      wg_barrier();
      float sum = 0.f, sq = 0.f;
      u32x2 pk[5];
#pragma unroll
      for (int t = 0; t < 5; ++t) {
        const f32x4 o = *(LAS f32x4*)(red + (w ^ 2) * 5120 + t * 1024 + lane * 16);
        const f32x4 mine = kh ? acc[1][t] : acc[0][t];
        pk[t] = pk4(unpk4(xres[t]) + ((mine + o) + unpk4(bb[t])));
        xres[t] = pk[t];
        const f32x4 xr = unpk4(pk[t]);
#pragma unroll
        for (int r = 0; r < 4; ++r) { sum += xr[r]; sq = __builtin_fmaf(xr[r], xr[r], sq); }
      }
      if (L == 0) {
        sum = xsum(sum); sq = xsum(sq);
        const unsigned xb = WS_X2 + mtE * 20480u + ((5 * cs8) >> 1) * 1024u;
        if ((cs8 & 1) == 0) {
          st16(cat2(pk[0], pk[1]), c.rws, lane * 16u, xb);
          st16(cat2(pk[2], pk[3]), c.rws, lane * 16u, xb + 1024u);
          st8(pk[4], c.rws, lane * 16u, xb + 2048u);
        } else {
          st8(pk[0], c.rws, lane * 16u + 8u, xb);
          st16(cat2(pk[1], pk[2]), c.rws, lane * 16u, xb + 1024u);
          st16(cat2(pk[3], pk[4]), c.rws, lane * 16u, xb + 2048u);
        }
        if (g == 0) { u32x2 v = {__builtin_bit_cast(unsigned, sum), __builtin_bit_cast(unsigned, sq)}; st8(v, c.rws, m * 64u + cs8 * 8u, WS_ST2 + mtE * 1024u); }
        wg_barrier();
        stage_qkv(c, rwqkv, false);
        if (w < 2) asm volatile("s_waitcnt vmcnt(38)" ::: "memory"); else asm volatile("s_waitcnt vmcnt(37)" ::: "memory");
        wg_barrier();
        set_flag(c, F_X2);
        TS(11);
      } else {
        const unsigned vo = (mtE * 16 + m) * 1280u + cs8 * 160u + g * 8u;
#pragma unroll
        for (int t = 0; t < 5; ++t) __builtin_amdgcn_raw_buffer_store_b64(pk[t], rout, vo, t * 32u, 0);
      }
    }
  }
  TS(23);
  if (b == 0) {
    GAS u64* arr = (GAS u64*)((char*)p.ws + WS_ARR) + threadIdx.x;
    unsigned spins = 0;
    while (!c.dead) {
      const u64 v = __hip_atomic_load(arr, __ATOMIC_RELAXED, __HIP_MEMORY_SCOPE_AGENT);
      if (__all(v == c.tag)) break;
      if (++spins > 40000u) c.dead = true;
      __builtin_amdgcn_s_sleep(1);
    }
    wg_barrier();
    if (threadIdx.x == 0) __hip_atomic_store((GAS u64*)((char*)p.ws + WS_Z), zval + 1, __ATOMIC_RELAXED, __HIP_MEMORY_SCOPE_AGENT);
  }
}

extern "C" void kernel_launch(void* const* d_in, const int* in_sizes, int n_in, void* d_out, int out_size, void* d_ws, size_t ws_size,
                              hipStream_t stream) {
  (void)in_sizes; (void)n_in; (void)out_size; (void)ws_size;
  Params p;
  p.x = d_in[0]; p.wqkv = d_in[1]; p.wo = d_in[2]; p.w1 = d_in[4]; p.b1 = d_in[5]; p.w2 = d_in[6]; p.b2 = d_in[7];
  p.out = d_out; p.ws = d_ws;
  hipFuncSetAttribute((const void*)fused_kernel, hipFuncAttributeMaxDynamicSharedMemorySize, LDS_BYTES);
  fused_kernel<<<256, 256, LDS_BYTES, stream>>>(p);
}
```

```cpp
#include <hip/hip_runtime.h>
#include <hip/hip_bf16.h>
#include <stdint.h>

#define DI __device__ __forceinline__
#define LAS __attribute__((address_space(3)))
#define GAS __attribute__((address_space(1)))

typedef __bf16 bf16x8 __attribute__((ext_vector_type(8)));
typedef __bf16 bf16x4 __attribute__((ext_vector_type(4)));
typedef __bf16 bf16x2 __attribute__((ext_vector_type(2)));
typedef float f32x4 __attribute__((ext_vector_type(4)));
typedef float f32x2 __attribute__((ext_vector_type(2)));
typedef unsigned u32x4 __attribute__((ext_vector_type(4)));
typedef unsigned u32x2 __attribute__((ext_vector_type(2)));
typedef unsigned long long u64;
typedef __amdgpu_buffer_rsrc_t rsrc_t;

constexpr unsigned WS_Z = 0;
constexpr unsigned WS_TMO = 256;
constexpr unsigned WS_ARR = 4096;
constexpr unsigned WS_FLG = 65536;
constexpr unsigned WS_TS = 96u << 20;
constexpr unsigned WS_PQ = 1u << 20;
constexpr unsigned WS_PO = WS_PQ + 16 * 153600;
constexpr unsigned WS_P1 = WS_PO + 8 * 102400;
constexpr unsigned WS_P2 = WS_P1 + 16 * 122880;
constexpr unsigned WS_KV = WS_P2 + 8 * 245760;
constexpr unsigned KV_WG = 22528;
constexpr unsigned KV_L = 16 * 16 * KV_WG;
constexpr unsigned XF_L = 128 * 20 * 1024;
constexpr unsigned HF_L = 128 * 48 * 1024;
constexpr unsigned ST_L = 2048 * 64;
constexpr unsigned WS_O = WS_KV + 2 * KV_L;
constexpr unsigned WS_X1 = WS_O + 2 * XF_L;
constexpr unsigned WS_XI = WS_X1 + 2 * XF_L;
constexpr unsigned WS_H = WS_XI + 2 * XF_L;
constexpr unsigned WS_ST1 = WS_H + 2 * HF_L;
constexpr unsigned WS_STI = WS_ST1 + 2 * ST_L;
constexpr unsigned WS_END = WS_STI + 2 * ST_L;

constexpr unsigned F_KV = 0, F_O = 2048, F_X1 = 4096, F_H = 6144;
constexpr unsigned F_X2 = 8192, F_X0 = 9216, F_PQ = 9472, F_P2 = 10112;

constexpr unsigned LDS_BYTES = 163840;
constexpr unsigned L_MISC = 153600;
constexpr unsigned L_CSQ = L_MISC;
constexpr unsigned L_CS1 = L_MISC + 512;
constexpr unsigned L_B1 = L_MISC + 1024;
constexpr unsigned L_B2 = L_MISC + 1536;
constexpr unsigned L_TS = L_MISC + 2048;
constexpr unsigned L_WOA = 67584;
constexpr unsigned L_REDO = 16384;
constexpr unsigned L_W1A = 36864;
constexpr unsigned L_RED2 = 133120;

struct Params {
  const void* x; const void* wqkv; const void* wo; const void* w1; const void* b1;
  const void* w2; const void* b2; void* out; void* ws;
};

DI rsrc_t mk_rsrc(const void* p, unsigned bytes) { return __builtin_amdgcn_make_buffer_rsrc((void*)p, 0, bytes, 0x00020000); }
DI void dma16(rsrc_t rs, unsigned lds_addr, unsigned voff, unsigned soff) {
  asm volatile("s_mov_b32 m0, %0\n\ts_nop 3\n\tbuffer_load_dwordx4 %1, %2, %3 offen lds"
               :: "s"(lds_addr), "v"(voff), "s"(rs), "s"(soff) : "memory");
}
DI void bar_lgkm() { asm volatile("s_waitcnt lgkmcnt(0)\n\ts_barrier" ::: "memory"); }
DI void bar_raw() { asm volatile("s_barrier" ::: "memory"); }
DI void wait_vm0() { asm volatile("s_waitcnt vmcnt(0)" ::: "memory"); }
DI u32x4 ld16(rsrc_t rs, unsigned voff, unsigned soff) { return __builtin_amdgcn_raw_buffer_load_b128(rs, voff, soff, 16); }
DI u32x2 ld8p(rsrc_t rs, unsigned voff, unsigned soff) { return __builtin_amdgcn_raw_buffer_load_b64(rs, voff, soff, 0); }
DI void st16(u32x4 v, rsrc_t rs, unsigned voff, unsigned soff) { __builtin_amdgcn_raw_buffer_store_b128(v, rs, voff + soff, 0, 16); }
DI void st8(u32x2 v, rsrc_t rs, unsigned voff, unsigned soff) { __builtin_amdgcn_raw_buffer_store_b64(v, rs, voff, soff, 16); }
DI bf16x4 tr_read(LAS char* p) { return __builtin_amdgcn_ds_read_tr16_b64_v4bf16((LAS bf16x4*)p); }
DI bf16x8 cat(bf16x4 a, bf16x4 b) { return __builtin_shufflevector(a, b, 0, 1, 2, 3, 4, 5, 6, 7); }
DI f32x4 mfma(bf16x8 a, bf16x8 b, f32x4 c) { return __builtin_amdgcn_mfma_f32_16x16x32_bf16(a, b, c, 0, 0, 0); }
DI bf16x8 asbf(u32x4 v) { return __builtin_bit_cast(bf16x8, v); }
DI u32x4 asu(bf16x8 v) { return __builtin_bit_cast(u32x4, v); }
DI unsigned pk2(float a, float b) { f32x2 v = {a, b}; return __builtin_bit_cast(unsigned, __builtin_convertvector(v, bf16x2)); }
DI u32x2 pk4(f32x4 v) { u32x2 r = {pk2(v[0], v[1]), pk2(v[2], v[3])}; return r; }
DI u32x4 cat2(u32x2 a, u32x2 b) { u32x4 r = {a[0], a[1], b[0], b[1]}; return r; }
DI float bflo(unsigned u) { return __builtin_bit_cast(float, u << 16); }
DI float bfhi(unsigned u) { return __builtin_bit_cast(float, u & 0xffff0000u); }
DI f32x4 unpk4(u32x2 v) { f32x4 r = {bflo(v[0]), bfhi(v[0]), bflo(v[1]), bfhi(v[1])}; return r; }
DI float xsum(float v) {
  unsigned u = __builtin_bit_cast(unsigned, v);
  auto a = __builtin_amdgcn_permlane16_swap(u, u, false, false);
  float s = __builtin_bit_cast(float, (unsigned)a[0]) + __builtin_bit_cast(float, (unsigned)a[1]);
  unsigned w = __builtin_bit_cast(unsigned, s);
  auto b = __builtin_amdgcn_permlane32_swap(w, w, false, false);
  return __builtin_bit_cast(float, (unsigned)b[0]) + __builtin_bit_cast(float, (unsigned)b[1]);
}
DI float max3(float a, float b, float c) {
#pragma float_control(precise, off)
  return __builtin_fmaxf(__builtin_fmaxf(a, b), c);
}
DI float xmax(float v) {
  unsigned u = __builtin_bit_cast(unsigned, v);
  auto a = __builtin_amdgcn_permlane16_swap(u, u, false, false);
  float s = max3(v, __builtin_bit_cast(float, (unsigned)a[0]), __builtin_bit_cast(float, (unsigned)a[1]));
  unsigned w = __builtin_bit_cast(unsigned, s);
  auto b = __builtin_amdgcn_permlane32_swap(w, w, false, false);
  return max3(s, __builtin_bit_cast(float, (unsigned)b[0]), __builtin_bit_cast(float, (unsigned)b[1]));
}
DI float bcast_row1(float v) {
  unsigned u = __builtin_bit_cast(unsigned, v);
  auto a = __builtin_amdgcn_permlane16_swap(u, u, false, false);
  unsigned w = a[1];
  auto b = __builtin_amdgcn_permlane32_swap(w, w, false, false);
  return __builtin_bit_cast(float, (unsigned)b[0]);
}
DI float dot2(unsigned a, unsigned b, float c) {
  return __builtin_amdgcn_fdot2_f32_bf16(__builtin_bit_cast(bf16x2, a), __builtin_bit_cast(bf16x2, b), c, false);
}

struct Ctx {
  LAS char* lds; unsigned lds0;
  rsrc_t rws;
  GAS char* flg;
  u64 tag;
  int rb, j, w, lane, m, g;
  bool dead;
  GAS unsigned* tmo;
};

DI void refresh(Ctx& c) {
  int l = c.lane, w = c.w, j = c.j, rb = c.rb; unsigned l0 = c.lds0;
  asm volatile("" : "+v"(l));
  asm volatile("" : "+s"(w), "+s"(j), "+s"(rb), "+s"(l0));
  c.lane = l; c.w = w; c.j = j; c.rb = rb; c.lds0 = l0;
  c.lds = (LAS char*)(size_t)l0;
  c.m = l & 15; c.g = l >> 4;
}
#define PHASE_VARS refresh(c); const int rb = c.rb, j = c.j, w = c.w, lane = c.lane, m = c.m, g = c.g; \
  const int hf = j & 1, cs8 = j >> 1, rg = w & 1, kh = w >> 1; const int mtE = 8 * rb + 4 * hf + 2 * rg + kh; \
  (void)hf; (void)cs8; (void)rg; (void)kh; (void)mtE; (void)lane; (void)m; (void)g; (void)rb;

DI void wait_flags(Ctx& c, unsigned first, int n, unsigned code) {
  GAS u64* p = (GAS u64*)(c.flg + (first + (unsigned)(c.lane < n ? c.lane : 0)) * 64u);
  unsigned spins = 0;
  while (!c.dead) {
    const u64 v = __hip_atomic_load(p, __ATOMIC_RELAXED, __HIP_MEMORY_SCOPE_AGENT);
    if (__all(v == c.tag)) break;
    if (++spins > 40000u) { c.dead = true; __hip_atomic_store(c.tmo, code, __ATOMIC_RELAXED, __HIP_MEMORY_SCOPE_AGENT); }
    __builtin_amdgcn_s_sleep(1);
  }
  asm volatile("" ::: "memory");
}
DI void publish(Ctx& c, unsigned idx) {
  wait_vm0();
  if (c.lane == 0) __hip_atomic_store((GAS u64*)(c.flg + idx * 64u), c.tag, __ATOMIC_RELAXED, __HIP_MEMORY_SCOPE_AGENT);
  asm volatile("" ::: "memory");
}

DI unsigned qkv_col(int cc, int j) { return (unsigned)(cc < 5 ? 40 * j + 8 * cc : (cc < 10 ? 640 + 40 * j + 8 * (cc - 5) : 1280 + 40 * j + 8 * (cc - 10))); }
struct TrLane { unsigned la0, la1, ls, lh; };
DI TrLane tr_lane(int l) {
  const int g = l >> 4, q = (l >> 2) & 3, pp = l & 3;
  TrLane t;
  t.la0 = (4 * g + q) * 64 + (2 * (g & 1) + (pp >> 1)) * 16 + (pp & 1) * 8;
  t.la1 = (4 * g + q) * 64 + (2 * (1 ^ (g & 1)) + (pp >> 1)) * 16 + (pp & 1) * 8;
  t.ls = (4 * g + q) * 32 + pp * 8;
  t.lh = (4 * g + q) * 16 + (pp & 1) * 8;
  return t;
}
DI u32x4 trf_pair(LAS char* b0, LAS char* b1, unsigned la) { return asu(cat(tr_read(b0 + la), tr_read(b1 + la))); }

DI void dma_run(rsrc_t rs, unsigned lds_addr, unsigned src, int n, int d, unsigned voff) {
  for (int p = d; p < n; p += 4) dma16(rs, lds_addr + p * 1024u, voff, src + p * 1024u);
}

template <int... Is> struct iseq {};
template <int N, int... Is> struct mk_iseq : mk_iseq<N - 1, N - 1, Is...> {};
template <int... Is> struct mk_iseq<0, Is...> { typedef iseq<Is...> type; };
template <int V> struct IC { static constexpr int value = V; };
constexpr int PD = 4;
template <int I, int NS, int NT, int WA, typename RD, typename LA, typename HK>
DI void gemm_group(f32x4 (&acc)[2][NT], u32x4 (&a)[2][NS], bf16x8 (&wf)[PD + 1], RD& rd, LA& la, HK& hk) {
  constexpr int N = NS * NT, s = I / NT, t = I % NT;
  hk(IC<I>());
  if constexpr (I + PD < N) wf[(I + PD) % (PD + 1)] = rd((I + PD) / NT, (I + PD) % NT);
  if constexpr (t == 0 && s + WA < NS) { a[0][s + WA] = la(0, s + WA); a[1][s + WA] = la(1, s + WA); }
  const f32x4 z4 = {0.f, 0.f, 0.f, 0.f};
  acc[0][t] = mfma(wf[I % (PD + 1)], asbf(a[0][s]), s == 0 ? z4 : acc[0][t]);
  acc[1][t] = mfma(wf[I % (PD + 1)], asbf(a[1][s]), s == 0 ? z4 : acc[1][t]);
  __builtin_amdgcn_sched_barrier(0);
}
template <int NS, int NT, int WA, typename RD, typename LA, typename HK, int... Is>
DI void gemm_steps_(f32x4 (&acc)[2][NT], u32x4 (&a)[2][NS], RD& rd, LA& la, HK& hk, iseq<Is...>) {
  bf16x8 wf[PD + 1];
  wf[0] = rd(0 / NT, 0 % NT); wf[1] = rd(1 / NT, 1 % NT); wf[2] = rd(2 / NT, 2 % NT); wf[3] = rd(3 / NT, 3 % NT);
  (gemm_group<Is, NS, NT, WA>(acc, a, wf, rd, la, hk), ...);
}
template <int NS, int NT, int WA, typename RD, typename LA, typename HK>
DI void gemm_steps(f32x4 (&acc)[2][NT], u32x4 (&a)[2][NS], RD rd, LA la, HK hk) {
  gemm_steps_<NS, NT, WA>(acc, a, rd, la, hk, typename mk_iseq<NS * NT>::type());
}
DI bf16x8 lds_frag(LAS char* p) { return *(LAS bf16x8*)p; }
DI LAS char* opaque(LAS char* p) { unsigned v = (unsigned)(size_t)p; asm volatile("" : "+v"(v)); return (LAS char*)(size_t)v; }

DI void row_stats(u32x4 st, float& mean, float& rstd) {
  const unsigned a0 = st[0], a1 = st[1], a2 = st[2], a3 = st[3];
  const float s = xsum(__builtin_bit_cast(float, a0) + __builtin_bit_cast(float, a2));
  const float q = xsum(__builtin_bit_cast(float, a1) + __builtin_bit_cast(float, a3));
  mean = s * (1.0f / 640.0f);
  const float var = fmaxf(q * (1.0f / 640.0f) - mean * mean, 0.0f);
  rstd = __builtin_amdgcn_rsqf(var + 1e-5f);
}

DI void attn_head(const Ctx& c, int h, int first_valid, const bf16x8 (&qf)[2], u32x2 (&omain)[2], u32x2 (&oleft)[2]) {
  const int g = c.g, m = c.m, l = c.lane;
  LAS char* base = c.lds + (2 * c.w) * 2816 + h * 1408;
  const int qa = h == 0 ? g : (g > 0 ? g - 1 : 0), qb = h == 0 ? 4 : (g < 1 ? 3 : 4);
  LAS char* ka = base + qa * 128 + m * 8;
  LAS char* kb = base + qb * 128 + m * 8;
  const int q4 = (l >> 2) & 3, p = l & 3;
  LAS char* v0 = base + 640 + (g >> 1) * 384 + p * 64 + (4 * (g & 1) + q4) * 8;
  LAS char* v1 = base + 640 + (g >> 1) * 384 + (4 + (p & 1)) * 64 + (4 * (g & 1) + q4) * 8;
  const float NEG = -1.0e30f;
  f32x4 S[2][17];
  const f32x4 z4 = {0.f, 0.f, 0.f, 0.f};
#pragma unroll
  for (int i = 0; i < 18; ++i) {
    const bf16x4 lo = *(LAS bf16x4*)(ka + i * 2816), hi = *(LAS bf16x4*)(kb + i * 2816);
    const bf16x8 kf = cat(lo, hi);
    if (i <= 16) S[0][i] = mfma(kf, qf[0], z4);
    if (i >= 1) S[1][i - 1] = mfma(kf, qf[1], z4);
  }
  if (first_valid > 0) {
    const f32x4 n4 = {NEG, NEG, NEG, NEG};
#pragma unroll
    for (int i = 0; i < 16; ++i)
      if (2 * c.w + i < first_valid) {
        S[0][i] = n4;
        if (i >= 1) S[1][i - 1] = n4;
      }
  }
  const float cs = 0.22360679774997896f * 1.4426950408889634f;
  const f32x2 cs2 = {cs, cs};
  bf16x8 P[2][9];
#pragma unroll
  for (int i = 0; i < 2; ++i) {
#pragma unroll
    for (int r = 0; r < 4; ++r) {
      S[i][0][r] = (4 * g + r > m) ? S[i][0][r] : NEG;
      S[i][16][r] = (4 * g + r <= m) ? S[i][16][r] : NEG;
    }
    float mx = NEG, my = NEG;
#pragma unroll
    for (int t = 0; t < 17; ++t) { mx = max3(mx, S[i][t][0], S[i][t][1]); my = max3(my, S[i][t][2], S[i][t][3]); }
    mx = xmax(max3(mx, my, my));
    const float mc = -mx * cs;
    const f32x2 mc2 = {mc, mc};
    unsigned pk[18][2];
#pragma unroll
    for (int t = 0; t < 17; ++t) {
      const f32x2 lo = {S[i][t][0], S[i][t][1]}, hi = {S[i][t][2], S[i][t][3]};
      const f32x2 a = __builtin_elementwise_fma(lo, cs2, mc2), b = __builtin_elementwise_fma(hi, cs2, mc2);
      pk[t + i][0] = pk2(__builtin_amdgcn_exp2f(a[0]), __builtin_amdgcn_exp2f(a[1]));
      pk[t + i][1] = pk2(__builtin_amdgcn_exp2f(b[0]), __builtin_amdgcn_exp2f(b[1]));
    }
    pk[i == 0 ? 17 : 0][0] = 0u; pk[i == 0 ? 17 : 0][1] = 0u;
#pragma unroll
    for (int st = 0; st < 9; ++st) { u32x4 v = {pk[2 * st][0], pk[2 * st][1], pk[2 * st + 1][0], pk[2 * st + 1][1]}; P[i][st] = asbf(v); }
  }
  f32x4 O[2][2];
#pragma unroll
  for (int st = 0; st < 9; ++st) {
    const bf16x8 vf0 = cat(tr_read(v0 + (2 * st) * 2816), tr_read(v0 + (2 * st + 1) * 2816));
    const bf16x8 vf1 = cat(tr_read(v1 + (2 * st) * 2816), tr_read(v1 + (2 * st + 1) * 2816));
#pragma unroll
    for (int i = 0; i < 2; ++i) {
      O[i][0] = mfma(vf0, P[i][st], st == 0 ? z4 : O[i][0]);
      O[i][1] = mfma(vf1, P[i][st], st == 0 ? z4 : O[i][1]);
    }
  }
#pragma unroll
  for (int i = 0; i < 2; ++i) {
    const float inv = __builtin_amdgcn_rcpf(bcast_row1(O[i][1][0]));
    omain[i] = pk4(O[i][0] * inv);
    oleft[i] = pk4(O[i][1] * inv);
  }
}

#define TSA(k) do {} while (0)
#define TS(k) do {} while (0)

DI void store_x(const u32x2 (&pk)[5], rsrc_t rws, int lane, int odd, unsigned xb) {
  if (!odd) {
    st16(cat2(pk[0], pk[1]), rws, lane * 16u, xb);
    st16(cat2(pk[2], pk[3]), rws, lane * 16u, xb + 1024u);
    st8(pk[4], rws, lane * 16u, xb + 2048u);
  } else {
    st8(pk[0], rws, lane * 16u + 8u, xb);
    st16(cat2(pk[1], pk[2]), rws, lane * 16u, xb + 1024u);
    st16(cat2(pk[3], pk[4]), rws, lane * 16u, xb + 2048u);
  }
}

__global__ void __launch_bounds__(512, 2) fused_kernel(Params p) {
  extern __shared__ __attribute__((aligned(16))) char smem[];
  Ctx c;
  c.lds = (LAS char*)smem;
  c.lds0 = (unsigned)(size_t)c.lds;
  const int b = blockIdx.x, xcd = b & 7, loc = b >> 3;
  c.rb = 4 * (xcd >> 1) + (loc >> 3); c.j = 8 * (xcd & 1) + (loc & 7);
  c.lane = threadIdx.x & 63;
  const int wv = __builtin_amdgcn_readfirstlane(threadIdx.x >> 6);
  c.w = wv & 3;
  c.rws = mk_rsrc(p.ws, WS_END);
  c.flg = (GAS char*)p.ws + WS_FLG;
  c.tmo = (GAS unsigned*)((char*)p.ws + WS_TMO);
  c.dead = false;
  TSA(0);
  u64 zval = *(const __attribute__((address_space(4))) u64*)((const char*)p.ws + WS_Z);
  const f32x4 z4 = {0.f, 0.f, 0.f, 0.f};

  if (wv < 4) {
    const rsrc_t rx = mk_rsrc(p.x, 2048u * 640u * 2u);
    u32x2 xres[5];
    {
      PHASE_VARS
      const unsigned u = 4u * b + w;
      if (u < 640u) {
        const rsrc_t rw = mk_rsrc(p.wqkv, 640u * 1920u * 2u);
        const int jj = u / 40u, s = (u % 40u) >> 1, h = u & 1u;
        const int r = lane >> 2, ch = (lane & 3) ^ (2 * ((lane >> 4) & 1));
        const unsigned stg = c.lds0 + w * 4096u, sb = s * (32u * 3840u);
        const TrLane t = tr_lane(lane);
        LAS char* sl = c.lds + w * 4096u;
        const unsigned dst = WS_PQ + jj * 153600u + s * 7680u;
        if (h == 0) {
          const unsigned v0 = r * 3840u + qkv_col(ch, jj) * 2u, v1 = r * 3840u + qkv_col(4 + ch, jj) * 2u;
          dma16(rw, stg, v0, sb); dma16(rw, stg + 1024u, v1, sb);
          dma16(rw, stg + 2048u, v0, sb + 16u * 3840u); dma16(rw, stg + 3072u, v1, sb + 16u * 3840u);
        } else {
          const unsigned v2 = r * 3840u + qkv_col(8 + ch, jj) * 2u;
          const unsigned vs = (lane >> 1) * 3840u + qkv_col(12 + (lane & 1), jj) * 2u;
          const unsigned vh = (lane & 31) * 3840u + qkv_col(14, jj) * 2u;
          dma16(rw, stg, v2, sb); dma16(rw, stg + 1024u, v2, sb + 16u * 3840u);
          dma16(rw, stg + 2048u, vs, sb); dma16(rw, stg + 3072u, vh, sb);
        }
        const unsigned vo = (mtE * 16 + m) * 1280u + cs8 * 160u + g * 8u;
#pragma unroll
        for (int e = 0; e < 5; ++e) xres[e] = ld8p(rx, vo, e * 32u);
        asm volatile("s_waitcnt vmcnt(5)" ::: "memory");
        if (h == 0) {
          const u32x4 f0 = trf_pair(sl, sl + 2048, t.la0), f1 = trf_pair(sl, sl + 2048, t.la1);
          const u32x4 f2 = trf_pair(sl + 1024, sl + 3072, t.la0), f3 = trf_pair(sl + 1024, sl + 3072, t.la1);
          st16(f0, c.rws, lane * 16u, dst); st16(f1, c.rws, lane * 16u, dst + 1024u);
          st16(f2, c.rws, lane * 16u, dst + 2048u); st16(f3, c.rws, lane * 16u, dst + 3072u);
        } else {
          const u32x4 f4 = trf_pair(sl, sl + 1024, t.la0), f5 = trf_pair(sl, sl + 1024, t.la1);
          const u32x4 f6 = trf_pair(sl + 2048, sl + 2560, t.ls), f7 = trf_pair(sl + 3072, sl + 3328, t.lh);
          st16(f4, c.rws, lane * 16u, dst + 4096u); st16(f5, c.rws, lane * 16u, dst + 5120u);
          st16(f6, c.rws, lane * 16u, dst + 6144u);
          if (m < 8) st16(f7, c.rws, g * 128u + m * 16u, dst + 7168u);
        }
      } else {
        u32x2 xl[10][2];
        const unsigned e = u - 640u, mt = (e >> 1) & 127u, hh = e & 1u;
        const bool live = u < 896u;
        if (live) {
#pragma unroll
          for (int s = 0; s < 10; ++s) {
            xl[s][0] = ld8p(rx, m * 1280u + g * 8u + s * 64u, mt * (16u * 1280u) + hh * 640u);
            xl[s][1] = ld8p(rx, m * 1280u + g * 8u + s * 64u + 32u, mt * (16u * 1280u) + hh * 640u);
          }
        }
        const unsigned vo = (mtE * 16 + m) * 1280u + cs8 * 160u + g * 8u;
#pragma unroll
        for (int t = 0; t < 5; ++t) xres[t] = ld8p(rx, vo, t * 32u);
        if (live) {
          float s0 = 0.f, q0 = 0.f;
#pragma unroll
          for (int s = 0; s < 10; ++s) {
            const u32x4 f = cat2(xl[s][0], xl[s][1]);
            st16(f, c.rws, lane * 16u, WS_XI + mt * 20480u + (10u * hh + s) * 1024u);
            const unsigned f0 = f[0], f1 = f[1], f2 = f[2], f3 = f[3];
            s0 = dot2(f0, 0x3f803f80u, s0); q0 = dot2(f0, f0, q0); s0 = dot2(f1, 0x3f803f80u, s0); q0 = dot2(f1, f1, q0);
            s0 = dot2(f2, 0x3f803f80u, s0); q0 = dot2(f2, f2, q0); s0 = dot2(f3, 0x3f803f80u, s0); q0 = dot2(f3, f3, q0);
          }
          s0 = xsum(s0); q0 = xsum(q0);
          u32x4 sv = {g == 0 ? __builtin_bit_cast(unsigned, s0) : 0u, g == 0 ? __builtin_bit_cast(unsigned, q0) : 0u, 0u, 0u};
          if (g < 2) st16(sv, c.rws, m * 64u + g * 16u, WS_STI + mt * 1024u + hh * 32u);
        }
      }
      asm volatile("" : "+s"(zval));
      c.tag = zval * 0x9E3779B97F4A7C15ull + 0x632BE59BD9B4E019ull;
      if (u < 896u) publish(c, u < 640u ? F_PQ + u : F_X0 + (u - 640u));
      TSA(1);
      bar_lgkm();
      TSA(2);
    }

#pragma unroll 1
    for (int L = 0; L < 2; ++L) {
      {
        PHASE_VARS
        TS(0);
        if (L == 0) wait_flags(c, F_X0 + (8 * rb + 2 * w) * 2, 4, 1); else wait_flags(c, F_X2 + (rb * 4 + w) * 16, 16, 2);
        TS(1);
        constexpr int WA = 12;
        u32x4 a[2][20], st[2];
        const unsigned xb = WS_XI + L * XF_L + (8 * rb + 2 * w) * 20480u;
        const unsigned l16 = lane * 16u;
        auto la = [&](int i, int s) __attribute__((always_inline)) { return ld16(c.rws, l16 + (s & 3) * 1024u, xb + i * 20480u + (s >> 2) * 4096u); };
#pragma unroll
        for (int s = 0; s < WA; ++s) { a[0][s] = la(0, s); a[1][s] = la(1, s); }
#pragma unroll
        for (int i = 0; i < 2; ++i) st[i] = ld16(c.rws, m * 64u + g * 16u, WS_STI + L * ST_L + (8 * rb + 2 * w + i) * 1024u);
        __builtin_amdgcn_sched_barrier(0);
        TS(2);
        bar_raw();
        TS(3);
        f32x4 acc[2][8];
        {
          LAS char* f0 = opaque(c.lds + l16); LAS char* f1 = opaque(c.lds + l16 + 61440); LAS char* f2 = opaque(c.lds + l16 + 122880);
          const unsigned hl = g * 128u + (m & 7) * 16u + 7168u;
          LAS char* h0 = opaque(c.lds + hl); LAS char* h1 = opaque(c.lds + hl + 61440); LAS char* h2 = opaque(c.lds + hl + 122880);
          gemm_steps<20, 8, WA>(acc, a,
            [&](int s, int t) __attribute__((always_inline)) {
              LAS char* fb = t < 7 ? (s < 8 ? f0 : (s < 16 ? f1 : f2)) : (s < 8 ? h0 : (s < 16 ? h1 : h2));
              return lds_frag(fb + (s & 7) * 7680 + (t < 7 ? t * 1024 : 0)); },
            la, [&](auto) __attribute__((always_inline)) {});
        }
        TS(4);
        float mean[2], rstd[2];
        row_stats(st[0], mean[0], rstd[0]); row_stats(st[1], mean[1], rstd[1]);
        bar_raw();
        TS(5);
        bf16x8 qf[2][2];
        {
          const unsigned vb = 640u + (m >> 3) * 384u + (m & 7) * 8u;
          unsigned ko[6];
          ko[0] = (unsigned)(g - 2) * 128u + m * 8u;
          ko[1] = g < 3 ? (2 + g) * 128u + m * 8u : 1408u + m * 8u;
          ko[2] = 1408u + (1 + g) * 128u + m * 8u;
          ko[3] = vb + g * 64u;
          ko[4] = g == 0 ? vb + 256u : 1408u + vb + (g - 1) * 64u;
          ko[5] = g < 2 ? 1408u + vb + (3 + g) * 64u : (g == 2 ? vb + 320u : 1408u + vb + 320u);
          const unsigned kvg = WS_KV + L * KV_L + (rb * 16 + j) * KV_WG;
#pragma unroll
          for (int i = 0; i < 2; ++i) {
            u32x2 pk[8];
#pragma unroll
            for (int t = 0; t < 8; ++t) {
              const f32x4 cs = *(LAS f32x4*)(c.lds + L_CSQ + (16 * t + 4 * g) * 4);
              pk[t] = pk4((acc[i][t] - mean[i] * cs) * rstd[i]);
            }
            const u32x2 zz = {0u, 0u};
            qf[0][i] = asbf(cat2(pk[0], g == 0 ? pk[1] : zz));
            qf[1][i] = asbf(cat2(g == 0 ? zz : pk[1], g < 2 ? pk[2] : zz));
            if (g >= 2) { pk[7][0] = 0x00003f80u; pk[7][1] = 0u; }
            const unsigned tl = 2 * w + i;
            LAS char* ld = c.lds + (16 + tl) * 2816;
#pragma unroll
            for (int t = 2; t < 8; ++t) {
              if (t > 2 || g >= 2) {
                st8(pk[t], c.rws, ko[t - 2], kvg + tl * 2816u);
                *(LAS u32x2*)(ld + ko[t - 2]) = pk[t];
              }
            }
          }
        }
        TS(6);
        publish(c, F_KV + L * 1024 + (rb * 16 + j) * 4 + w);
        TS(7);
        {
          u32x4 hv[2][6];
          if (rb >= 1) {
            GAS u64* fp = (GAS u64*)(c.flg + (F_KV + L * 1024 + ((rb - 1 - (rb >= 2 ? (lane & 1) : 0)) * 16 + j) * 4 + w) * 64u);
            unsigned spins = 0;
            while (!c.dead) {
              const u64 v = __hip_atomic_load(fp, __ATOMIC_RELAXED, __HIP_MEMORY_SCOPE_AGENT);
              if (__all(v == c.tag)) break;
              if (++spins > 40000u) { c.dead = true; __hip_atomic_store(c.tmo, 3u, __ATOMIC_RELAXED, __HIP_MEMORY_SCOPE_AGENT); }
              __builtin_amdgcn_s_sleep(1);
            }
            asm volatile("" ::: "memory");
          }
          TS(8);
#pragma unroll
          for (int e = 0; e < 2; ++e) {
            const int srb = rb - 2 + e;
            const u32x4 z = {0u, 0u, 0u, 0u};
            if (srb >= 0) {
              const unsigned sb = WS_KV + L * KV_L + (srb * 16 + j) * KV_WG + w * 5632u;
#pragma unroll
              for (int i = 0; i < 6; ++i) hv[e][i] = ld16(c.rws, (i < 5 || lane < 32) ? l16 + i * 1024u : 0u, sb);
            } else {
#pragma unroll
              for (int i = 0; i < 6; ++i) hv[e][i] = z;
            }
          }
#pragma unroll
          for (int e = 0; e < 2; ++e)
#pragma unroll
            for (int i = 0; i < 6; ++i)
              if (i < 5 || lane < 32) *(LAS u32x4*)(c.lds + e * KV_WG + w * 5632u + i * 1024u + l16) = hv[e][i];
        }
        TS(9);
        bar_lgkm();
        TS(10);
        u32x2 om[2][2], ol[2][2];
#pragma unroll 1
        for (int h = 0; h < 2; ++h) {
          bf16x8 q2[2] = {h ? qf[1][0] : qf[0][0], h ? qf[1][1] : qf[0][1]};
          u32x2 a0[2], a1[2];
          attn_head(c, h, 16 - 8 * rb, q2, a0, a1);
          if (h == 0) { om[0][0] = a0[0]; om[0][1] = a0[1]; ol[0][0] = a1[0]; ol[0][1] = a1[1]; }
          else { om[1][0] = a0[0]; om[1][1] = a0[1]; ol[1][0] = a1[0]; ol[1][1] = a1[1]; }
        }
        TS(11);
#pragma unroll
        for (int i = 0; i < 2; ++i) {
          const unsigned mt = 8 * rb + 2 * w + i, ob = WS_O + L * XF_L + mt * 20480u;
          st16(cat2(om[0][i], om[1][i]), c.rws, l16, ob + j * 1024u);
          if (g == 0) st16(cat2(ol[0][i], ol[1][i]), c.rws, m * 16u, ob + (16 + (j >> 2)) * 1024u + (j & 3) * 256u);
        }
        publish(c, F_O + L * 1024 + (rb * 4 + w) * 16 + j);
        TS(12);
        bar_raw();
        TS(13);
      }
      {
        PHASE_VARS
        u32x4 a[2][10];
        wait_flags(c, F_O + L * 1024 + (rb * 4 + 2 * hf + rg) * 16 + 8 * kh, 8, 4);
        TS(14);
        const unsigned l16 = lane * 16u;
        const unsigned ob = WS_O + L * XF_L + (8 * rb + 4 * hf + 2 * rg) * 20480u;
        auto la = [&](int i, int s) __attribute__((always_inline)) {
          return ld16(c.rws, l16 + (s < 8 ? (s & 3) : (s - 8)) * 1024u, ob + i * 20480u + (s < 8 ? (8 * kh + (s & 4)) : 16 + 2 * kh) * 1024u); };
#pragma unroll
        for (int s = 0; s < 10; ++s) { a[0][s] = la(0, s); a[1][s] = la(1, s); }
        __builtin_amdgcn_sched_barrier(0);
        TS(15);
        bar_raw();
        TS(16);
        f32x4 acc[2][5];
        {
          LAS char* fa = opaque(c.lds + l16 + L_WOA + kh * 43008); LAS char* fb = opaque(c.lds + l16 + kh * 8192);
          gemm_steps<10, 5, 10>(acc, a,
            [&](int s, int t) __attribute__((always_inline)) { const int q = 5 * s + t; return lds_frag(q < 42 ? fa + q * 1024 : fb + (q - 42) * 1024); },
            la, [&](auto) __attribute__((always_inline)) {});
        }
        TS(17);
        LAS char* red = c.lds + L_REDO + l16;
#pragma unroll
        for (int t = 0; t < 5; ++t) *(LAS f32x4*)(red + w * 5120 + t * 1024) = kh ? acc[0][t] : acc[1][t];
        bar_lgkm();
        TS(18);
        float sum = 0.f, sq = 0.f;
        u32x2 pk[5];
#pragma unroll
        for (int t = 0; t < 5; ++t) {
          const f32x4 o = *(LAS f32x4*)(red + (w ^ 2) * 5120 + t * 1024);
          const f32x4 mine = kh ? acc[1][t] : acc[0][t];
          pk[t] = pk4(unpk4(xres[t]) + (mine + o));
          xres[t] = pk[t];
          const f32x4 xr = unpk4(pk[t]);
#pragma unroll
          for (int r = 0; r < 4; ++r) { sum += xr[r]; sq = __builtin_fmaf(xr[r], xr[r], sq); }
        }
        store_x(pk, c.rws, lane, cs8 & 1, WS_X1 + L * XF_L + mtE * 20480u + ((5 * cs8) >> 1) * 1024u);
        sum = xsum(sum); sq = xsum(sq);
        if (g == 0) { u32x2 v = {__builtin_bit_cast(unsigned, sum), __builtin_bit_cast(unsigned, sq)}; st8(v, c.rws, m * 64u + cs8 * 8u, WS_ST1 + L * ST_L + mtE * 1024u); }
        TS(19);
        publish(c, F_X1 + L * 1024 + (rb * 4 + 2 * hf + rg) * 16 + 2 * cs8 + kh);
        TS(20);
      }
      {
        PHASE_VARS
        wait_flags(c, F_X1 + L * 1024 + (rb * 4 + w) * 16, 16, 5);
        TS(21);
        constexpr int WA = 12;
        u32x4 a[2][20], st[2];
        const unsigned xb = WS_X1 + L * XF_L + (8 * rb + 2 * w) * 20480u;
        const unsigned l16 = lane * 16u;
        auto la = [&](int i, int s) __attribute__((always_inline)) { return ld16(c.rws, l16 + (s & 3) * 1024u, xb + i * 20480u + (s >> 2) * 4096u); };
#pragma unroll
        for (int s = 0; s < WA; ++s) { a[0][s] = la(0, s); a[1][s] = la(1, s); }
#pragma unroll
        for (int i = 0; i < 2; ++i) st[i] = ld16(c.rws, m * 64u + g * 16u, WS_ST1 + L * ST_L + (8 * rb + 2 * w + i) * 1024u);
        __builtin_amdgcn_sched_barrier(0);
        TS(22);
        bar_raw();
        TS(23);
        f32x4 acc[2][6];
        {
          LAS char* f0 = opaque(c.lds + l16 + L_W1A); LAS char* f1 = opaque(c.lds + l16 + L_W1A + 61440); LAS char* f2 = opaque(c.lds + l16);
          gemm_steps<20, 6, WA>(acc, a,
            [&](int s, int t) __attribute__((always_inline)) {
              const int q = 6 * s + t;
              return lds_frag(q < 60 ? f0 + q * 1024 : (q < 114 ? f1 + (q - 60) * 1024 : f2 + (q - 114) * 1024)); },
            la, [&](auto) __attribute__((always_inline)) {});
        }
        TS(24);
        float mean[2], rstd[2];
        row_stats(st[0], mean[0], rstd[0]); row_stats(st[1], mean[1], rstd[1]);
        bar_raw();
        TS(25);
#pragma unroll
        for (int i = 0; i < 2; ++i) {
          u32x2 pk[6];
#pragma unroll
          for (int t = 0; t < 6; ++t) {
            const f32x4 cs = *(LAS f32x4*)(c.lds + L_CS1 + (16 * t + 4 * g) * 4);
            const f32x4 bb = *(LAS f32x4*)(c.lds + L_B1 + (16 * t + 4 * g) * 4);
            f32x4 v = (acc[i][t] - mean[i] * cs) * rstd[i] + bb;
#pragma unroll
            for (int r = 0; r < 4; ++r) { const float x = fmaxf(v[r], 0.f); v[r] = x * x; }
            pk[t] = pk4(v);
          }
          const unsigned hb = WS_H + L * HF_L + (8 * rb + 2 * w + i) * 49152u + 3 * j * 1024u;
#pragma unroll
          for (int u = 0; u < 3; ++u) st16(cat2(pk[2 * u], pk[2 * u + 1]), c.rws, l16, hb + u * 1024u);
        }
        TS(26);
        publish(c, F_H + L * 1024 + (rb * 4 + w) * 16 + j);
        TS(27);
      }
      {
        PHASE_VARS
        wait_flags(c, F_H + L * 1024 + (rb * 4 + 2 * hf + rg) * 16 + 8 * kh, 8, 6);
        TS(28);
        constexpr int WA = 16;
        u32x4 a[2][24];
        const unsigned l16 = lane * 16u;
        const unsigned hb = WS_H + L * HF_L + (8 * rb + 4 * hf + 2 * rg) * 49152u + 24 * kh * 1024u;
        auto la = [&](int i, int s) __attribute__((always_inline)) { return ld16(c.rws, l16 + (s & 3) * 1024u, hb + i * 49152u + (s >> 2) * 4096u); };
#pragma unroll
        for (int s = 0; s < WA; ++s) { a[0][s] = la(0, s); a[1][s] = la(1, s); }
        __builtin_amdgcn_sched_barrier(0);
        TS(29);
        bar_raw();
        TS(30);
        f32x4 acc[2][5];
        {
          LAS char* f0 = opaque(c.lds + l16 + kh * 76800); LAS char* f1 = opaque(c.lds + l16 + kh * 76800 + 40960);
          gemm_steps<24, 5, WA>(acc, a,
            [&](int s, int t) __attribute__((always_inline)) {
              const int sl = s < 15 ? s : s - 15;
              return lds_frag(sl < 8 ? f0 + sl * 5120 + t * 1024 : f1 + (sl - 8) * 5120 + t * 1024); },
            la,
            [&](auto ic) __attribute__((always_inline)) {
              constexpr int I = decltype(ic)::value;
              if constexpr (I == 25 || I == 50 || I == 75 - PD || I == 100 - PD) bar_raw();
            });
        }
        TS(31);
        LAS char* red = c.lds + L_RED2 + l16;
#pragma unroll
        for (int t = 0; t < 5; ++t) *(LAS f32x4*)(red + w * 5120 + t * 1024) = kh ? acc[0][t] : acc[1][t];
        bar_lgkm();
        TS(32);
        float sum = 0.f, sq = 0.f;
        u32x2 pk[5];
#pragma unroll
        for (int t = 0; t < 5; ++t) {
          const f32x4 o = *(LAS f32x4*)(red + (w ^ 2) * 5120 + t * 1024);
          const f32x4 bb = *(LAS f32x4*)(c.lds + L_B2 + (16 * t + 4 * g) * 4);
          const f32x4 mine = kh ? acc[1][t] : acc[0][t];
          pk[t] = pk4(unpk4(xres[t]) + ((mine + o) + bb));
          xres[t] = pk[t];
          const f32x4 xr = unpk4(pk[t]);
#pragma unroll
          for (int r = 0; r < 4; ++r) { sum += xr[r]; sq = __builtin_fmaf(xr[r], xr[r], sq); }
        }
        if (L == 0) {
          store_x(pk, c.rws, lane, cs8 & 1, WS_XI + XF_L + mtE * 20480u + ((5 * cs8) >> 1) * 1024u);
          sum = xsum(sum); sq = xsum(sq);
          if (g == 0) { u32x2 v = {__builtin_bit_cast(unsigned, sum), __builtin_bit_cast(unsigned, sq)}; st8(v, c.rws, m * 64u + cs8 * 8u, WS_STI + ST_L + mtE * 1024u); }
          TS(33);
          publish(c, F_X2 + (rb * 4 + 2 * hf + rg) * 16 + 2 * cs8 + kh);
          TS(34);
          bar_lgkm();
        } else {
          const rsrc_t rout = mk_rsrc(p.out, 2048u * 640u * 2u);
          const unsigned vo = (mtE * 16 + m) * 1280u + cs8 * 160u + g * 8u;
#pragma unroll
          for (int t = 0; t < 5; ++t) __builtin_amdgcn_raw_buffer_store_b64(pk[t], rout, vo, t * 32u, 0);
        }
      }
    }
  } else {
    const int d = c.w;
    {
      PHASE_VARS
      const unsigned v = 4u * b + d;
      const unsigned stg = c.lds0 + 16384u + d * 6144u;
      LAS char* sl = c.lds + 16384u + d * 6144u;
      const int r = lane >> 2, ch = (lane & 3) ^ (2 * ((lane >> 4) & 1));
      const TrLane t = tr_lane(lane);
      if (v >= 160u && v < 480u) {
        const rsrc_t rw = mk_rsrc(p.w1, 640u * 1536u * 2u);
        const unsigned e = v - 160u, jj = e / 20u, s = e % 20u;
        const unsigned vp = r * 3072u + jj * 192u + ch * 16u, sb = s * (32u * 3072u);
#pragma unroll
        for (int h = 0; h < 2; ++h)
#pragma unroll
          for (int u = 0; u < 3; ++u) dma16(rw, stg + (h * 3 + u) * 1024u, vp, sb + h * (16u * 3072u) + u * 64u);
        wait_vm0();
        const unsigned dst = WS_P1 + jj * 122880u + s * 6144u;
#pragma unroll
        for (int u = 0; u < 6; ++u)
          st16(trf_pair(sl + (u >> 1) * 1024, sl + (3 + (u >> 1)) * 1024, (u & 1) ? t.la1 : t.la0), c.rws, lane * 16u, dst + u * 1024u);
      } else if (v < 864u) {
        const bool wo = v < 160u;
        const rsrc_t rw = mk_rsrc(wo ? p.wo : p.w2, wo ? 640u * 640u * 2u : 1536u * 640u * 2u);
        const unsigned e = wo ? v : v - 480u, c8 = wo ? e / 20u : e / 48u, s = wo ? e % 20u : e % 48u;
        const unsigned cb = c8 * 160u;
        const int rho = lane >> 1, r2 = rho & 15;
        unsigned vp, vs, sb, tadv, dst;
        if (!wo) {
          vp = r * 1280u + cb + ch * 16u; vs = rho * 1280u + cb + 128u + (lane & 1) * 16u;
          sb = s * (32u * 1280u); tadv = 16u * 1280u; dst = WS_P2 + c8 * 245760u + s * 5120u;
        } else if (s < 16u) {
          vp = r * 1280u + cb + ch * 16u; vs = (20 * (rho >> 4) + r2) * 1280u + cb + 128u + (lane & 1) * 16u;
          sb = s * (40u * 1280u); tadv = 20u * 1280u; dst = WS_PO + c8 * 102400u + (s >> 3) * 51200u + (s & 7u) * 5120u;
        } else {
          vp = (40 * (r >> 2) + (r & 3)) * 1280u + cb + ch * 16u;
          vs = (40 * (r2 >> 2) + 20 * (rho >> 4) + (r2 & 3)) * 1280u + cb + 128u + (lane & 1) * 16u;
          sb = (160u * (s - 16u) + 16u) * 1280u; tadv = 20u * 1280u;
          dst = WS_PO + c8 * 102400u + ((s - 16u) >> 1) * 51200u + (8u + (s & 1u)) * 5120u;
        }
        sb = __builtin_amdgcn_readfirstlane(sb); tadv = __builtin_amdgcn_readfirstlane(tadv); dst = __builtin_amdgcn_readfirstlane(dst);
        dma16(rw, stg, vp, sb); dma16(rw, stg + 1024u, vp, sb + 64u);
        dma16(rw, stg + 2048u, vp, sb + tadv); dma16(rw, stg + 3072u, vp, sb + tadv + 64u);
        dma16(rw, stg + 4096u, vs, sb);
        wait_vm0();
#pragma unroll
        for (int u = 0; u < 4; ++u)
          st16(trf_pair(sl + (u >> 1) * 1024, sl + (2 + (u >> 1)) * 1024, (u & 1) ? t.la1 : t.la0), c.rws, lane * 16u, dst + u * 1024u);
        st16(trf_pair(sl + 4096, sl + 4608, t.ls), c.rws, lane * 16u, dst + 4096u);
      }
      if (d == 0 && lane < 48) {
        const unsigned bb = __builtin_amdgcn_raw_buffer_load_b32(mk_rsrc(p.b1, 1536u * 2u), lane * 4u, j * 192u, 0);
        f32x2 o = {bflo(bb), bfhi(bb)};
        *(LAS f32x2*)(c.lds + L_B1 + lane * 8) = o;
      }
      if (d == 1 && lane < 40) {
        const unsigned bb = __builtin_amdgcn_raw_buffer_load_b32(mk_rsrc(p.b2, 640u * 2u), lane * 4u, cs8 * 160u, 0);
        f32x2 o = {bflo(bb), bfhi(bb)};
        *(LAS f32x2*)(c.lds + L_B2 + lane * 8) = o;
      }
      asm volatile("" : "+s"(zval));
      c.tag = zval * 0x9E3779B97F4A7C15ull + 0x632BE59BD9B4E019ull;
      if (v < 864u) publish(c, F_P2 + v);
      TSA(1);
      bar_lgkm();
      TSA(2);
      if (d == 0 && lane == 0)
        __hip_atomic_store((GAS u64*)((char*)p.ws + WS_ARR) + b, c.tag, __ATOMIC_RELAXED, __HIP_MEMORY_SCOPE_AGENT);
      wait_flags(c, F_PQ + j * 40, 40, 7);
      TSA(3);
      dma_run(c.rws, c.lds0, WS_PQ + j * 153600u, 150, d, lane * 16u);
      TSA(4);
    }
#pragma unroll 1
    for (int L = 0; L < 2; ++L) {
      PHASE_VARS
      const unsigned l16 = lane * 16u;
      wait_vm0();
      TS(3);
      bar_raw();
      if (L == 0) {
#pragma unroll 1
        for (int e = 0; e < 2; ++e) {
          const int t = d + 4 * e;
          LAS char* fp = c.lds + (t < 7 ? t * 1024u + l16 : 7168u + g * 128u + (m & 7) * 16u);
          float s0 = 0.f, s1 = 0.f;
#pragma unroll
          for (int s = 0; s < 20; ++s) {
            const u32x4 f = *(LAS u32x4*)(fp + s * 7680);
            const unsigned f0 = f[0], f1 = f[1], f2 = f[2], f3 = f[3];
            s0 = dot2(f0, 0x3f803f80u, s0); s1 = dot2(f1, 0x3f803f80u, s1); s0 = dot2(f2, 0x3f803f80u, s0); s1 = dot2(f3, 0x3f803f80u, s1);
          }
          s0 = xsum(s0 + s1);
          if (g == 0) *(LAS float*)(c.lds + L_CSQ + (16 * t + m) * 4) = s0;
        }
        asm volatile("s_waitcnt lgkmcnt(0)" ::: "memory");
      }
      bar_raw();
      bar_raw();
      if (L == 0) wait_flags(c, F_P2 + cs8 * 20, 20, 8);
      dma_run(c.rws, c.lds0 + L_WOA, WS_PO + cs8 * 102400u, 42, d, l16);
      dma_run(c.rws, c.lds0 + L_WOA + 43008u, WS_PO + cs8 * 102400u + 51200u, 42, d, l16);
      if (L == 0) { wait_flags(c, F_P2 + 160 + j * 20, 20, 9); wait_flags(c, F_P2 + 480 + cs8 * 48, 48, 10); }
      TS(11);
      bar_raw();
      dma_run(c.rws, c.lds0, WS_PO + cs8 * 102400u + 42u * 1024u, 8, d, l16);
      dma_run(c.rws, c.lds0 + 8192u, WS_PO + cs8 * 102400u + 51200u + 42u * 1024u, 8, d, l16);
      dma_run(c.rws, c.lds0 + L_W1A, WS_P1 + j * 122880u, 28, d, l16);
      asm volatile("s_waitcnt vmcnt(7)" ::: "memory");
      TS(16);
      bar_raw();
      bar_raw();
      dma_run(c.rws, c.lds0 + L_W1A + 28u * 1024u, WS_P1 + j * 122880u + 28u * 1024u, 86, d, l16);
      dma_run(c.rws, c.lds0, WS_P1 + j * 122880u + 114u * 1024u, 6, d, l16);
      wait_vm0();
      TS(23);
      bar_raw();
      if (L == 0) {
#pragma unroll 1
        for (int e = 0; e < 2; ++e) {
          const int t = d + 4 * e;
          if (t < 6) {
            float s0 = 0.f, s1 = 0.f;
#pragma unroll
            for (int s = 0; s < 20; ++s) {
              const int q0 = 6 * s;
              const unsigned off = (q0 + t < 114) ? L_W1A + (q0 + t) * 1024u : (q0 + t - 114) * 1024u;
              const u32x4 f = *(LAS u32x4*)(c.lds + off + l16);
              const unsigned f0 = f[0], f1 = f[1], f2 = f[2], f3 = f[3];
              s0 = dot2(f0, 0x3f803f80u, s0); s1 = dot2(f1, 0x3f803f80u, s1); s0 = dot2(f2, 0x3f803f80u, s0); s1 = dot2(f3, 0x3f803f80u, s1);
            }
            s0 = xsum(s0 + s1);
            if (g == 0) *(LAS float*)(c.lds + L_CS1 + (16 * t + m) * 4) = s0;
          }
        }
        asm volatile("s_waitcnt lgkmcnt(0)" ::: "memory");
      }
      bar_raw();
      const unsigned w2s = WS_P2 + cs8 * 245760u;
      dma_run(c.rws, c.lds0, w2s, 75, d, l16);
      dma_run(c.rws, c.lds0 + 76800u, w2s + 122880u, 75, d, l16);
      wait_vm0();
      TS(30);
      bar_raw();
      bar_raw();
      dma_run(c.rws, c.lds0, w2s + 75u * 1024u, 25, d, l16);
      dma_run(c.rws, c.lds0 + 76800u, w2s + 122880u + 75u * 1024u, 25, d, l16);
      bar_raw();
      dma_run(c.rws, c.lds0 + 25600u, w2s + 100u * 1024u, 20, d, l16);
      dma_run(c.rws, c.lds0 + 76800u + 25600u, w2s + 122880u + 100u * 1024u, 20, d, l16);
      asm volatile("s_waitcnt vmcnt(10)" ::: "memory");
      bar_raw();
      wait_vm0();
      bar_raw();
      bar_raw();
      if (L == 0) {
        dma_run(c.rws, c.lds0, WS_PQ + j * 153600u, 130, d, l16);
        bar_raw();
        dma_run(c.rws, c.lds0 + L_RED2, WS_PQ + j * 153600u + L_RED2, 20, d, l16);
      }
    }
    if (b == 0 && d == 0) {
      GAS u64* arr = (GAS u64*)((char*)p.ws + WS_ARR) + c.lane;
      unsigned spins = 0;
      while (!c.dead) {
        const u64 v0 = __hip_atomic_load(arr, __ATOMIC_RELAXED, __HIP_MEMORY_SCOPE_AGENT);
        const u64 v1 = __hip_atomic_load(arr + 64, __ATOMIC_RELAXED, __HIP_MEMORY_SCOPE_AGENT);
        const u64 v2 = __hip_atomic_load(arr + 128, __ATOMIC_RELAXED, __HIP_MEMORY_SCOPE_AGENT);
        const u64 v3 = __hip_atomic_load(arr + 192, __ATOMIC_RELAXED, __HIP_MEMORY_SCOPE_AGENT);
        if (__all(v0 == c.tag && v1 == c.tag && v2 == c.tag && v3 == c.tag)) break;
        if (++spins > 40000u) c.dead = true;
        __builtin_amdgcn_s_sleep(1);
      }
      if (c.lane == 0) __hip_atomic_store((GAS u64*)((char*)p.ws + WS_Z), zval + 1, __ATOMIC_RELAXED, __HIP_MEMORY_SCOPE_AGENT);
    }
  }
}

extern "C" void kernel_launch(void* const* d_in, const int* in_sizes, int n_in, void* d_out, int out_size, void* d_ws, size_t ws_size,
                              hipStream_t stream) {
  (void)in_sizes; (void)n_in; (void)out_size; (void)ws_size;
  Params p;
  p.x = d_in[0]; p.wqkv = d_in[1]; p.wo = d_in[2]; p.w1 = d_in[4]; p.b1 = d_in[5]; p.w2 = d_in[6]; p.b2 = d_in[7];
  p.out = d_out; p.ws = d_ws;
  (void)hipFuncSetAttribute((const void*)fused_kernel, hipFuncAttributeMaxDynamicSharedMemorySize, LDS_BYTES);
  fused_kernel<<<256, 512, LDS_BYTES, stream>>>(p);
}
```

```cpp
#include <hip/hip_runtime.h>
#include <hip/hip_bf16.h>
#include <stdint.h>

#define DI __device__ __forceinline__
#define LAS __attribute__((address_space(3)))
#define GAS __attribute__((address_space(1)))

typedef __bf16 bf16x8 __attribute__((ext_vector_type(8)));
typedef __bf16 bf16x4 __attribute__((ext_vector_type(4)));
typedef __bf16 bf16x2 __attribute__((ext_vector_type(2)));
typedef float f32x4 __attribute__((ext_vector_type(4)));
typedef float f32x2 __attribute__((ext_vector_type(2)));
typedef unsigned u32x4 __attribute__((ext_vector_type(4)));
typedef unsigned u32x2 __attribute__((ext_vector_type(2)));
typedef unsigned long long u64;
typedef __amdgpu_buffer_rsrc_t rsrc_t;

constexpr unsigned WS_Z = 0;
constexpr unsigned WS_TMO = 256;
constexpr unsigned WS_ARR = 4096;
constexpr unsigned WS_FLG = 64u << 20;
constexpr unsigned WS_TS = 96u << 20;
constexpr unsigned WS_PQ = 1u << 20;
constexpr unsigned WS_PO = WS_PQ + 16 * 153600;
constexpr unsigned WS_P1 = WS_PO + 8 * 102400;
constexpr unsigned WS_P2 = WS_P1 + 16 * 122880;
constexpr unsigned WS_KV = WS_P2 + 8 * 245760;
constexpr unsigned KV_WG = 22528;
constexpr unsigned KV_L = 16 * 16 * KV_WG;
constexpr unsigned XF_L = 128 * 20 * 1024;
constexpr unsigned HF_L = 128 * 48 * 1024;
constexpr unsigned ST_L = 2048 * 64;
constexpr unsigned WS_O = WS_KV + 2 * KV_L;
constexpr unsigned WS_X1 = WS_O + 2 * XF_L;
constexpr unsigned WS_XI = WS_X1 + 2 * XF_L;
constexpr unsigned WS_H = WS_XI + 2 * XF_L;
constexpr unsigned WS_ST1 = WS_H + 2 * HF_L;
constexpr unsigned WS_STI = WS_ST1 + 2 * ST_L;
constexpr unsigned WS_CPQ = WS_STI + 2 * ST_L;
constexpr unsigned WS_CP1 = WS_CPQ + 16 * 20 * 512;
constexpr unsigned WS_END = WS_CP1 + 16 * 20 * 384;

#define S_KV 128u
#define S_HOT 128u
#define S_PRE 128u
#define G_HOT (16u * S_HOT)
#define POLL_SLEEP 1
struct Flg { unsigned off, stride; };
constexpr unsigned FB_KV = 0, FB_O = FB_KV + 2048 * S_KV, FB_X1 = FB_O + 128 * G_HOT, FB_H = FB_X1 + 128 * G_HOT, FB_X2 = FB_H + 128 * G_HOT;
constexpr unsigned FB_X0 = FB_X2 + 64 * G_HOT, FB_PQ = FB_X0 + 1024 * S_PRE, FB_P2 = FB_PQ + 640 * S_PRE, FB_END = FB_P2 + 864 * S_PRE;
static_assert(FB_END <= (16u << 20), "tag words");
#define F_KV(i) Flg{FB_KV + (unsigned)(i) * S_KV, S_KV}
#define F_O(i) Flg{FB_O + ((unsigned)(i) >> 4) * G_HOT + ((unsigned)(i) & 15u) * S_HOT, S_HOT}
#define F_X1(i) Flg{FB_X1 + ((unsigned)(i) >> 4) * G_HOT + ((unsigned)(i) & 15u) * S_HOT, S_HOT}
#define F_H(i) Flg{FB_H + ((unsigned)(i) >> 4) * G_HOT + ((unsigned)(i) & 15u) * S_HOT, S_HOT}
#define F_X2(i) Flg{FB_X2 + ((unsigned)(i) >> 4) * G_HOT + ((unsigned)(i) & 15u) * S_HOT, S_HOT}
#define F_X0(i) Flg{FB_X0 + (unsigned)(i) * S_PRE, S_PRE}
#define F_PQ(i) Flg{FB_PQ + (unsigned)(i) * S_PRE, S_PRE}
#define F_P2(i) Flg{FB_P2 + (unsigned)(i) * S_PRE, S_PRE}

constexpr unsigned LDS_BYTES = 163840;
constexpr unsigned L_MISC = 153600;
constexpr unsigned L_CSQ = L_MISC;
constexpr unsigned L_CS1 = L_MISC + 512;
constexpr unsigned L_B1 = L_MISC + 1024;
constexpr unsigned L_B2 = L_MISC + 1536;
constexpr unsigned L_CNT = L_MISC + 1920;
constexpr unsigned L_STG = L_MISC + 2048;
constexpr unsigned L_PSTG = 137216;
constexpr unsigned L_QW = 102400;
constexpr unsigned L_WOA = 71680;
constexpr unsigned L_REDO = 20480;
constexpr unsigned L_W1A = 40960;
constexpr unsigned L_RED2 = 133120;

struct Params {
  const void* x; const void* wqkv; const void* wo; const void* w1; const void* b1;
  const void* w2; const void* b2; void* out; void* ws;
};

#define KVPUB_AT 10
#define WA_W1 10
#define WA_W2 12
#define W2B_SLOW 8
#define W2B_SLEEP 3
#define W1_BAR (6 * (19 - WA_W1) + 1)
#define RQ_A 20
#define RQ_B 12
#define BA_CNT 0
#define R1_EARLY 2
#define P2_PEEK 1
#define CS1_LATE 0
#define R1N 2
#define NORM_LAST 1
#define W2_EARLY 8
#define PQ_SLEEP 3
#define OUT_AUX 0

#define PRE_SLEEP 14
#define PRE_Q PRE_SLEEP
#define PRE_O PRE_SLEEP
#define PRE_W1 PRE_SLEEP
#define PRE_W2 PRE_SLEEP
#define DP_O 1
#define DP_X1 1
#define DP_H 1
#define DP_X2 1
#define DPRE 14
#define DPRE_Q DPRE
#define DPRE_O 17
#define DPRE_W1 17
#define DPRE_W2 21
#define INLOOP 0
#define SPEC 0
#define PZ_H 0
#define BQ0_EARLY 1
#define PZ_START 1
#define PZ_NAP 0
#define PZ_ATT 1
#define EQ_FIRST 13
#define PRETOUCH 0
#define RU_AT 1
#define MID_FIRST 4
#define HALO_SLEEP0 0
#define B1Q 1
#define EQ_ISSUE 25
#define X_NAP 0
#define HALO_SLEEP (B1Q ? 4 : 30)
#define PQ_PRE 25
#define X0_PRE 10
#define X0_PRE2 60
#define SLOW_SLEEP 8
#define ATT2 1
#define DBG_H 1
#define FORCE_EXACT 0
#define POLL_DEPTH 1
#define POLL_GAP 4

DI rsrc_t mk_rsrc(const void* p, unsigned bytes) { return __builtin_amdgcn_make_buffer_rsrc((void*)p, 0, bytes, 0x00020000); }
DI void dma16(rsrc_t rs, unsigned lds_addr, unsigned voff, unsigned soff) {
  asm volatile("s_mov_b32 m0, %0\n\ts_nop 3\n\tbuffer_load_dwordx4 %1, %2, %3 offen sc1 lds"
               :: "s"(lds_addr), "v"(voff), "s"(rs), "s"(soff) : "memory");
}
DI void bar_lgkm() { asm volatile("s_waitcnt lgkmcnt(0)\n\ts_barrier" ::: "memory"); }
DI void bar_raw() { asm volatile("s_barrier" ::: "memory"); }
DI void wait_vm0() { asm volatile("s_waitcnt vmcnt(0)" ::: "memory"); }
DI u32x4 ld16(rsrc_t rs, unsigned voff, unsigned soff) { return __builtin_amdgcn_raw_buffer_load_b128(rs, voff, soff, 16); }
DI u32x4 ld16p(rsrc_t rs, unsigned voff, unsigned soff) { return __builtin_amdgcn_raw_buffer_load_b128(rs, voff, soff, 0); }
DI void lds16(LAS char* p, u32x4 v) { *(LAS u32x4*)p = v; }
DI void pin() { asm volatile("" ::: "memory"); __builtin_amdgcn_sched_barrier(0); }
DI u32x2 ld8p(rsrc_t rs, unsigned voff, unsigned soff) { return __builtin_amdgcn_raw_buffer_load_b64(rs, voff, soff, 0); }
DI void st16(u32x4 v, rsrc_t rs, unsigned voff, unsigned soff) { __builtin_amdgcn_raw_buffer_store_b128(v, rs, voff + soff, 0, 16); }
DI void st8(u32x2 v, rsrc_t rs, unsigned voff, unsigned soff) { __builtin_amdgcn_raw_buffer_store_b64(v, rs, voff, soff, 16); }
DI bf16x4 tr_read(LAS char* p) { return __builtin_amdgcn_ds_read_tr16_b64_v4bf16((LAS bf16x4*)p); }
DI bf16x8 cat(bf16x4 a, bf16x4 b) { return __builtin_shufflevector(a, b, 0, 1, 2, 3, 4, 5, 6, 7); }
DI f32x4 mfma(bf16x8 a, bf16x8 b, f32x4 c) { return __builtin_amdgcn_mfma_f32_16x16x32_bf16(a, b, c, 0, 0, 0); }
DI bf16x8 asbf(u32x4 v) { return __builtin_bit_cast(bf16x8, v); }
DI u32x4 asu(bf16x8 v) { return __builtin_bit_cast(u32x4, v); }
DI unsigned pk2(float a, float b) { f32x2 v = {a, b}; return __builtin_bit_cast(unsigned, __builtin_convertvector(v, bf16x2)); }
DI u32x2 pk4(f32x4 v) { u32x2 r = {pk2(v[0], v[1]), pk2(v[2], v[3])}; return r; }
DI u32x4 cat2(u32x2 a, u32x2 b) { u32x4 r = {a[0], a[1], b[0], b[1]}; return r; }
DI float bflo(unsigned u) { return __builtin_bit_cast(float, u << 16); }
DI float bfhi(unsigned u) { return __builtin_bit_cast(float, u & 0xffff0000u); }
DI f32x4 unpk4(u32x2 v) { f32x4 r = {bflo(v[0]), bfhi(v[0]), bflo(v[1]), bfhi(v[1])}; return r; }
DI float xsum(float v) {
  unsigned u = __builtin_bit_cast(unsigned, v);
  auto a = __builtin_amdgcn_permlane16_swap(u, u, false, false);
  float s = __builtin_bit_cast(float, (unsigned)a[0]) + __builtin_bit_cast(float, (unsigned)a[1]);
  unsigned w = __builtin_bit_cast(unsigned, s);
  auto b = __builtin_amdgcn_permlane32_swap(w, w, false, false);
  return __builtin_bit_cast(float, (unsigned)b[0]) + __builtin_bit_cast(float, (unsigned)b[1]);
}
DI float max3(float a, float b, float c) {
#pragma float_control(precise, off)
  return __builtin_fmaxf(__builtin_fmaxf(a, b), c);
}
DI float xmax(float v) {
  unsigned u = __builtin_bit_cast(unsigned, v);
  auto a = __builtin_amdgcn_permlane16_swap(u, u, false, false);
  float s = max3(v, __builtin_bit_cast(float, (unsigned)a[0]), __builtin_bit_cast(float, (unsigned)a[1]));
  unsigned w = __builtin_bit_cast(unsigned, s);
  auto b = __builtin_amdgcn_permlane32_swap(w, w, false, false);
  return max3(s, __builtin_bit_cast(float, (unsigned)b[0]), __builtin_bit_cast(float, (unsigned)b[1]));
}
DI float bcast_row1(float v) {
  unsigned u = __builtin_bit_cast(unsigned, v);
  auto a = __builtin_amdgcn_permlane16_swap(u, u, false, false);
  unsigned w = a[1];
  auto b = __builtin_amdgcn_permlane32_swap(w, w, false, false);
  return __builtin_bit_cast(float, (unsigned)b[0]);
}
DI float dot2(unsigned a, unsigned b, float c) {
  return __builtin_amdgcn_fdot2_f32_bf16(__builtin_bit_cast(bf16x2, a), __builtin_bit_cast(bf16x2, b), c, false);
}

constexpr unsigned POISON = 0xffffffffu;
DI unsigned san(unsigned v) { return __builtin_elementwise_min(v, POISON - 1u); }
DI u32x2 san2(u32x2 v) { const unsigned a = v[0], b = v[1]; return u32x2{san(a), san(b)}; }
DI u32x4 san4(u32x4 v) { const unsigned a = v[0], b = v[1], c = v[2], d = v[3]; return u32x4{san(a), san(b), san(c), san(d)}; }
DI unsigned umax(unsigned a, unsigned b) { return __builtin_elementwise_max(a, b); }
DI unsigned umax3(unsigned a, unsigned b, unsigned c) { return umax(umax(a, b), c); }
DI unsigned umax4(unsigned m, u32x4 v) { const unsigned a = v[0], b = v[1], c = v[2], d = v[3]; return umax3(umax3(m, a, b), c, d); }
DI bool any_poison(unsigned mx) { return __builtin_amdgcn_ballot_w64(mx == POISON) != 0; }
DI u32x2 ld8(rsrc_t rs, unsigned voff, unsigned soff) { return __builtin_amdgcn_raw_buffer_load_b64(rs, voff, soff, 16); }

DI void acc_stats(u32x2 v, float& sum, float& sq) {
  const unsigned a = v[0], b = v[1];
  sum = dot2(a, 0x3f803f80u, dot2(b, 0x3f803f80u, sum));
  sq = dot2(a, a, dot2(b, b, sq));
}

#define TMO_NOTE(code) (void)(code)
struct Ctx {
  LAS char* lds; unsigned lds0;
  rsrc_t rws;
  GAS char* flg;
  u64 tag;
  int rb, j, w, lane, m, g;
  bool dead;
  GAS unsigned* tmo;
};

DI unsigned touch(rsrc_t r, unsigned off) { return __builtin_amdgcn_raw_buffer_load_b32(r, 0, off, 0); }
DI void drop(unsigned v) { asm volatile("" :: "v"(v)); }
DI void refresh(Ctx& c) {
  int l = c.lane, w = c.w, j = c.j, rb = c.rb; unsigned l0 = c.lds0;
  asm volatile("" : "+v"(l));
  asm volatile("" : "+s"(w), "+s"(j), "+s"(rb), "+s"(l0));
  c.lane = l; c.w = w; c.j = j; c.rb = rb; c.lds0 = l0;
  c.lds = (LAS char*)(size_t)l0;
  c.m = l & 15; c.g = l >> 4;
}
#define PHASE_VARS refresh(c); const int rb = c.rb, j = c.j, w = c.w, lane = c.lane, m = c.m, g = c.g; \
  const int hf = j & 1, cs8 = j >> 1, rg = w & 1, kh = w >> 1; const int mtE = 8 * rb + 4 * hf + 2 * rg + kh; \
  (void)hf; (void)cs8; (void)rg; (void)kh; (void)mtE; (void)lane; (void)m; (void)g; (void)rb;

DI u64 ld_flag(GAS u64* p) { return __hip_atomic_load(p, __ATOMIC_RELAXED, __HIP_MEMORY_SCOPE_AGENT); }
template <int N> DI void nap() {
  if constexpr (N > 127) { __builtin_amdgcn_s_sleep(127); nap<N - 127>(); }
  else if constexpr (N > 0) __builtin_amdgcn_s_sleep(N);
}
template <int INTERVAL = POLL_SLEEP>
DI void poll_tag(Ctx& c, GAS u64* p, unsigned code) {
  unsigned spins = 0;
  while (!c.dead) {
    const u64 v = ld_flag(p);
    if (__all(v == c.tag)) break;
    if (++spins > 40000u) { c.dead = true; TMO_NOTE(code); }
    __builtin_amdgcn_s_sleep(INTERVAL);
  }
  asm volatile("" ::: "memory");
}
struct Junk { u64 j0, j1, j2; };
DI void hold(const Junk&) {}
DI void poll_fast(Ctx& c, GAS u64* p, unsigned code, Junk& j) {
  j.j0 = j.j1 = j.j2 = 0;
  poll_tag(c, p, code);
}
DI unsigned blk_of(int rb, unsigned j) {
  return (((unsigned)rb & 3u) * 8u + (j & 7u)) * 8u + 2u * ((unsigned)rb >> 2) + (j >> 3);
}
DI GAS u64* flag_ptr(const Ctx& c, Flg f, int n) { return (GAS u64*)(c.flg + f.off + (unsigned)(c.lane < n ? c.lane : 0) * f.stride); }
DI void wait_flags(Ctx& c, Flg first, int n, unsigned code) { poll_tag<SLOW_SLEEP>(c, flag_ptr(c, first, n), code); }
template <int PRE>
DI void wait_fast(Ctx& c, Flg first, int n, unsigned code, Junk& j) {
  nap<PRE>();
  poll_fast(c, flag_ptr(c, first, n), code, j);
}
DI void give_up(Ctx& c, unsigned& spins, unsigned code) {
  if (++spins > 40000u) { c.dead = true; TMO_NOTE(code); }
}
template <int PRE>
DI void wait_probe(Ctx& c, unsigned voff, unsigned soff, unsigned code) {
  nap<PRE>();
  unsigned spins = 0;
  while (!c.dead) {
    asm volatile("" ::: "memory");
    const unsigned v = __builtin_amdgcn_raw_buffer_load_b32(c.rws, voff, soff, 16);
    if (!any_poison(v)) break;
    give_up(c, spins, code);
    __builtin_amdgcn_s_sleep(POLL_SLEEP);
  }
  asm volatile("" ::: "memory");
}
DI void load_stats(Ctx& c, u32x4 (&st)[2], unsigned base) {
  const unsigned vo = c.m * 8u + c.g * 256u;
  st[0] = cat2(ld8(c.rws, vo, base), ld8(c.rws, vo + 128u, base));
  st[1] = cat2(ld8(c.rws, vo, base + 1024u), ld8(c.rws, vo + 128u, base + 1024u));
}
template <int PRE>
DI void wait_stats(Ctx& c, u32x4 (&st)[2], unsigned base, unsigned code) {
  nap<PRE>();
  unsigned spins = 0;
  const unsigned vo = c.m * 8u + c.g * 256u;
  for (;;) {
    asm volatile("" ::: "memory");
    st[0] = cat2(ld8(c.rws, vo, base), ld8(c.rws, vo + 128u, base));
    st[1] = cat2(ld8(c.rws, vo, base + 1024u), ld8(c.rws, vo + 128u, base + 1024u));
    if (c.dead || !any_poison(umax4(umax4(0u, st[0]), st[1]))) break;
    give_up(c, spins, code);
    __builtin_amdgcn_s_sleep(POLL_SLEEP);
  }
  asm volatile("" ::: "memory");
}
DI void publish(Ctx& c, Flg f) {
  wait_vm0();
  u64 t = c.tag;
  asm volatile("" : "+s"(t));
  if (c.lane == 0) __hip_atomic_store((GAS u64*)(c.flg + f.off), t, __ATOMIC_RELAXED, __HIP_MEMORY_SCOPE_AGENT);
  asm volatile("" ::: "memory");
}

DI unsigned qkv_col(int cc, int j) { return (unsigned)(cc < 5 ? 640 + 40 * j + 8 * cc : (cc < 10 ? 1280 + 40 * j + 8 * (cc - 5) : 40 * j + 8 * (cc - 10))); }
struct TrLane { unsigned la0, la1, ls, lh; };
DI TrLane tr_lane(int l) {
  const int g = l >> 4, q = (l >> 2) & 3, pp = l & 3;
  TrLane t;
  t.la0 = (4 * g + q) * 64 + (2 * (g & 1) + (pp >> 1)) * 16 + (pp & 1) * 8;
  t.la1 = (4 * g + q) * 64 + (2 * (1 ^ (g & 1)) + (pp >> 1)) * 16 + (pp & 1) * 8;
  t.ls = (4 * g + q) * 32 + pp * 8;
  t.lh = (4 * g + q) * 16 + (pp & 1) * 8;
  return t;
}
DI u32x4 trf_pair(LAS char* b0, LAS char* b1, unsigned la) { return asu(cat(tr_read(b0 + la), tr_read(b1 + la))); }

DI float tile_colsum(u32x4 f) {
  const unsigned f0 = f[0], f1 = f[1], f2 = f[2], f3 = f[3];
  return xsum(dot2(f0, 0x3f803f80u, dot2(f1, 0x3f803f80u, 0.f)) + dot2(f2, 0x3f803f80u, dot2(f3, 0x3f803f80u, 0.f)));
}
DI unsigned sel4(int g, float a, float b, float c, float d) { return __builtin_bit_cast(unsigned, g == 0 ? a : (g == 1 ? b : (g == 2 ? c : d))); }
DI void gather_load(float (&v)[10], rsrc_t rws, unsigned base, unsigned pitch, int lane) {
#pragma unroll
  for (int e = 0; e < 10; ++e)
    v[e] = __builtin_bit_cast(float, __builtin_amdgcn_raw_buffer_load_b32(rws, (lane & 31) * 4u + (lane >> 5) * 10u * pitch + e * pitch, base, 16));
}
DI void gather_load_raw(float (&v)[10], rsrc_t rws, unsigned base, unsigned pitch, int lane) {
#pragma unroll
  for (int e = 0; e < 10; ++e)
    asm volatile("buffer_load_dword %0, %1, %2, %3 offen sc1" : "=v"(v[e]) : "v"((lane & 31) * 4u + (lane >> 5) * 10u * pitch + e * pitch), "s"(rws), "s"(base) : "memory");
}
DI void landed(float (&v)[10]) {
  asm volatile("" : "+v"(v[0]), "+v"(v[1]), "+v"(v[2]), "+v"(v[3]), "+v"(v[4]), "+v"(v[5]), "+v"(v[6]), "+v"(v[7]), "+v"(v[8]), "+v"(v[9]));
}
DI float gather_sum(const float (&v)[10]) {
  float s = ((v[0] + v[1]) + (v[2] + v[3])) + ((v[4] + v[5]) + (v[6] + v[7])) + (v[8] + v[9]);
  const unsigned u = __builtin_bit_cast(unsigned, s);
  auto b = __builtin_amdgcn_permlane32_swap(u, u, false, false);
  return __builtin_bit_cast(float, (unsigned)b[0]) + __builtin_bit_cast(float, (unsigned)b[1]);
}

DI void dma_run(rsrc_t rs, unsigned lds_addr, unsigned src, int n, int d, unsigned voff) {
  for (int p = d; p < n; p += 4) dma16(rs, lds_addr + p * 1024u, voff, src + p * 1024u);
}

template <int... Is> struct iseq {};
template <int N, int... Is> struct mk_iseq : mk_iseq<N - 1, N - 1, Is...> {};
template <int... Is> struct mk_iseq<0, Is...> { typedef iseq<Is...> type; };
template <int V> struct IC { static constexpr int value = V; };
constexpr int PD = 4;
template <typename LA>
DI void refetch(Ctx& c, u32x4& a0, u32x4& a1, LA& la, int s) {
  unsigned spins = 0;
  while (!c.dead) {
    __builtin_amdgcn_s_sleep(2);
    asm volatile("" ::: "memory");
    a0 = la(0, s); a1 = la(1, s);
    if (!any_poison(umax4(umax4(0u, a0), a1))) break;
    give_up(c, spins, 20);
  }
  asm volatile("" ::: "memory");
}
struct Chk { unsigned mx; u64 bad; };
template <int I, int NS, int NT, int WA, bool CHK, typename RD, typename LA, typename HK>
DI void gemm_group(Ctx& c, Chk& k, f32x4 (&acc)[2][NT], u32x4 (&a)[2][NS], bf16x8 (&wf)[PD + 1], RD& rd, LA& la, HK& hk) {
  constexpr int N = NS * NT, s = I / NT, t = I % NT;
  hk(IC<I>());
  if constexpr (I + PD < N) wf[(I + PD) % (PD + 1)] = rd((I + PD) / NT, (I + PD) % NT);
  if constexpr (t == 0 && s + WA < NS) { a[0][s + WA] = la(0, s + WA); a[1][s + WA] = la(1, s + WA); }
  if constexpr (CHK && s + 1 < NS) {
    const u32x4 p = a[0][s + 1], q = a[1][s + 1];
    const unsigned p0 = p[0], p1 = p[1], p2 = p[2], p3 = p[3], q0 = q[0], q1 = q[1], q2 = q[2], q3 = q[3];
    if constexpr (t == 0) k.mx = umax3(p0, p1, p2);
    if constexpr (t == 1) k.mx = umax3(k.mx, p3, q0);
    if constexpr (t == 2) k.mx = umax3(k.mx, q1, q2);
    if constexpr (t == 3) k.bad = __builtin_amdgcn_ballot_w64(umax(k.mx, q3) == POISON);
    if constexpr (t == 4) if (__builtin_expect(k.bad != 0, 0)) refetch(c, a[0][s + 1], a[1][s + 1], la, s + 1);
  }
  const f32x4 z4 = {0.f, 0.f, 0.f, 0.f};
  acc[0][t] = mfma(wf[I % (PD + 1)], asbf(a[0][s]), s == 0 ? z4 : acc[0][t]);
  acc[1][t] = mfma(wf[I % (PD + 1)], asbf(a[1][s]), s == 0 ? z4 : acc[1][t]);
  __builtin_amdgcn_sched_barrier(0);
}
template <int NS, int NT, int WA, bool CHK, typename RD, typename LA, typename HK, int... Is>
DI void gemm_steps_(Ctx& c, f32x4 (&acc)[2][NT], u32x4 (&a)[2][NS], RD& rd, LA& la, HK& hk, iseq<Is...>) {
  bf16x8 wf[PD + 1];
  wf[0] = rd(0 / NT, 0 % NT); wf[1] = rd(1 / NT, 1 % NT); wf[2] = rd(2 / NT, 2 % NT); wf[3] = rd(3 / NT, 3 % NT);
  Chk k = {0u, 0ull};
  if constexpr (CHK) if (__builtin_expect(any_poison(umax4(umax4(0u, a[0][0]), a[1][0])), 0)) refetch(c, a[0][0], a[1][0], la, 0);
  (gemm_group<Is, NS, NT, WA, CHK>(c, k, acc, a, wf, rd, la, hk), ...);
}
template <int NS, int NT, int WA, bool CHK, typename RD, typename LA, typename HK>
DI void gemm_steps(Ctx& c, f32x4 (&acc)[2][NT], u32x4 (&a)[2][NS], RD rd, LA la, HK hk) {
  gemm_steps_<NS, NT, WA, CHK>(c, acc, a, rd, la, hk, typename mk_iseq<NS * NT>::type());
}
DI bool has_nan(f32x4 a0, f32x4 a1) {
  const float x = a0[0], y = a1[0];
  return __builtin_amdgcn_ballot_w64(x != x || y != y) != 0;
}
template <int NS, int NT, typename LA>
DI void redo_gemm(Ctx& c, f32x4 (&acc)[2][NT], LA& la, unsigned wbase) {
  const f32x4 z4 = {0.f, 0.f, 0.f, 0.f};
#pragma unroll
  for (int t = 0; t < NT; ++t) { acc[0][t] = z4; acc[1][t] = z4; }
#pragma unroll 1
  for (int s = 0; s < NS; ++s) {
    u32x4 a0 = la(0, s), a1 = la(1, s);
    if (any_poison(umax4(umax4(0u, a0), a1))) refetch(c, a0, a1, la, s);
#pragma unroll
    for (int t = 0; t < NT; ++t) {
      const bf16x8 wf = asbf(ld16(c.rws, c.lane * 16u + t * 1024u, wbase + s * (NT * 1024u)));
      acc[0][t] = mfma(wf, asbf(a0), acc[0][t]); acc[1][t] = mfma(wf, asbf(a1), acc[1][t]);
    }
  }
}
DI bf16x8 lds_frag(LAS char* p) { return *(LAS bf16x8*)p; }
DI LAS char* opaque(LAS char* p) { unsigned v = (unsigned)(size_t)p; asm volatile("" : "+v"(v)); return (LAS char*)(size_t)v; }

DI void row_stats(u32x4 st, float& mean, float& rstd) {
  const unsigned a0 = st[0], a1 = st[1], a2 = st[2], a3 = st[3];
  const float s = xsum(__builtin_bit_cast(float, a0) + __builtin_bit_cast(float, a2));
  const float q = xsum(__builtin_bit_cast(float, a1) + __builtin_bit_cast(float, a3));
  mean = s * (1.0f / 640.0f);
  const float var = fmaxf(q * (1.0f / 640.0f) - mean * mean, 0.0f);
  rstd = __builtin_amdgcn_rsqf(var + 1e-5f);
}

DI void row_stats2(const u32x4 (&st)[2], float (&mean)[2], float (&rstd)[2]) {
  row_stats(st[0], mean[0], rstd[0]); row_stats(st[1], mean[1], rstd[1]);
  asm volatile("" : "+v"(mean[0]), "+v"(mean[1]), "+v"(rstd[0]), "+v"(rstd[1]));
  __builtin_amdgcn_sched_barrier(0);
}

DI void attn_head(const Ctx& c, int h, int first_valid, const bf16x8 (&qf)[2], u32x2 (&omain)[2], u32x2 (&oleft)[2]) {
  const int g = c.g, m = c.m, l = c.lane;
  LAS char* base = c.lds + (2 * c.w) * 2816 + h * 1408;
  const int qa = h == 0 ? g : (g > 0 ? g - 1 : 0), qb = h == 0 ? 4 : (g < 1 ? 3 : 4);
  LAS char* ka = base + qa * 128 + m * 8;
  LAS char* kb = base + qb * 128 + m * 8;
  const int q4 = (l >> 2) & 3, p = l & 3;
  LAS char* v0 = base + 640 + (g >> 1) * 384 + p * 64 + (4 * (g & 1) + q4) * 8;
  LAS char* v1 = base + 640 + (g >> 1) * 384 + (4 + (p & 1)) * 64 + (4 * (g & 1) + q4) * 8;
  const float NEG = -1.0e30f;
  f32x4 S[2][17];
  const f32x4 z4 = {0.f, 0.f, 0.f, 0.f};
#pragma unroll
  for (int i = 0; i < 18; ++i) {
    const bf16x4 lo = *(LAS bf16x4*)(ka + i * 2816), hi = *(LAS bf16x4*)(kb + i * 2816);
    const bf16x8 kf = cat(lo, hi);
    if (i <= 16) S[0][i] = mfma(kf, qf[0], z4);
    if (i >= 1) S[1][i - 1] = mfma(kf, qf[1], z4);
  }
  if (first_valid > 0) {
    const f32x4 n4 = {NEG, NEG, NEG, NEG};
#pragma unroll
    for (int i = 0; i < 16; ++i)
      if (2 * c.w + i < first_valid) {
        S[0][i] = n4;
        if (i >= 1) S[1][i - 1] = n4;
      }
  }
  const float cs = 0.22360679774997896f * 1.4426950408889634f;
  const f32x2 cs2 = {cs, cs};
  bf16x8 P[2][9];
#pragma unroll
  for (int i = 0; i < 2; ++i) {
#pragma unroll
    for (int r = 0; r < 4; ++r) {
      S[i][0][r] = (4 * g + r > m) ? S[i][0][r] : NEG;
      S[i][16][r] = (4 * g + r <= m) ? S[i][16][r] : NEG;
    }
    float mx = NEG, my = NEG;
#pragma unroll
    for (int t = 0; t < 17; ++t) { mx = max3(mx, S[i][t][0], S[i][t][1]); my = max3(my, S[i][t][2], S[i][t][3]); }
    mx = xmax(max3(mx, my, my));
    const float mc = -mx * cs;
    const f32x2 mc2 = {mc, mc};
    unsigned pk[18][2];
#pragma unroll
    for (int t = 0; t < 17; ++t) {
      const f32x2 lo = {S[i][t][0], S[i][t][1]}, hi = {S[i][t][2], S[i][t][3]};
      const f32x2 a = __builtin_elementwise_fma(lo, cs2, mc2), b = __builtin_elementwise_fma(hi, cs2, mc2);
      pk[t + i][0] = pk2(__builtin_amdgcn_exp2f(a[0]), __builtin_amdgcn_exp2f(a[1]));
      pk[t + i][1] = pk2(__builtin_amdgcn_exp2f(b[0]), __builtin_amdgcn_exp2f(b[1]));
    }
    pk[i == 0 ? 17 : 0][0] = 0u; pk[i == 0 ? 17 : 0][1] = 0u;
#pragma unroll
    for (int st = 0; st < 9; ++st) { u32x4 v = {pk[2 * st][0], pk[2 * st][1], pk[2 * st + 1][0], pk[2 * st + 1][1]}; P[i][st] = asbf(v); }
  }
  f32x4 O[2][2];
#pragma unroll
  for (int st = 0; st < 9; ++st) {
    const bf16x8 vf0 = cat(tr_read(v0 + (2 * st) * 2816), tr_read(v0 + (2 * st + 1) * 2816));
    const bf16x8 vf1 = cat(tr_read(v1 + (2 * st) * 2816), tr_read(v1 + (2 * st + 1) * 2816));
#pragma unroll
    for (int i = 0; i < 2; ++i) {
      O[i][0] = mfma(vf0, P[i][st], st == 0 ? z4 : O[i][0]);
      O[i][1] = mfma(vf1, P[i][st], st == 0 ? z4 : O[i][1]);
    }
  }
#pragma unroll
  for (int i = 0; i < 2; ++i) {
    const float inv = __builtin_amdgcn_rcpf(bcast_row1(O[i][1][0]));
    omain[i] = pk4(O[i][0] * inv);
    oleft[i] = pk4(O[i][1] * inv);
  }
}

DI void mfma_first(f32x4& d, bf16x8 a, u32x4 b) { asm volatile("s_nop 1\n\tv_mfma_f32_16x16x32_bf16 %0, %1, %2, 0" : "=&v"(d) : "v"(a), "v"(b)); }
DI void mfma_last(f32x4& d, bf16x8 a, u32x4 b) { asm volatile("s_nop 1\n\tv_mfma_f32_16x16x32_bf16 %0, %1, %2, %0" : "+v"(d) : "v"(a), "v"(b)); }
DI void mfma_more(f32x4& d, bf16x8 a, u32x4 b) { asm volatile("v_mfma_f32_16x16x32_bf16 %0, %1, %2, %0" : "+v"(d) : "v"(a), "v"(b)); }
template <typename F, int... Is> DI void sfor_(F& f, iseq<Is...>) { (f(IC<Is>()), ...); }
template <int N, typename F> DI void sfor(F f) { sfor_(f, typename mk_iseq<N>::type()); }
struct AttnAddr { LAS char* ka; LAS char* kb; LAS char* v0; LAS char* v1; };
DI AttnAddr attn_addr(const Ctx& c, int h) {
  const int g = c.g, m = c.m, l = c.lane;
  LAS char* base = c.lds + (2 * c.w) * 2816 + h * 1408;
  const int qa = h == 0 ? g : (g > 0 ? g - 1 : 0), qb = h == 0 ? 4 : (g < 1 ? 3 : 4);
  const int q4 = (l >> 2) & 3, p = l & 3;
  AttnAddr a;
  a.ka = base + qa * 128 + m * 8;
  a.kb = g < 2 ? base + qb * 128 + m * 8 : base + 960 + (m & 8) * 48 + (m & 7) * 8;
  a.v0 = base + 640 + (g >> 1) * 384 + p * 64 + (4 * (g & 1) + q4) * 8;
  a.v1 = base + 640 + (g >> 1) * 384 + (4 + (p & 1)) * 64 + (4 * (g & 1) + q4) * 8;
  return a;
}
template <typename TSF, typename CHK>
DI void attn_head2(TSF tsf, CHK unsafe, const Ctx& c, const AttnAddr& ad, const bf16x8 (&qf)[2], u32x2 (&omain)[2], u32x2 (&oleft)[2]) {
  const int g = c.g, m = c.m;
  LAS char* const ka = ad.ka; LAS char* const kb = ad.kb; LAS char* const v0 = ad.v0; LAS char* const v1 = ad.v1;
  float NEG;
  asm volatile("v_mov_b32 %0, 0xf149f2ca" : "=v"(NEG));
  const f32x4 z4 = {0.f, 0.f, 0.f, 0.f};
  auto old_edge = [&](f32x4 s) __attribute__((always_inline)) {
    f32x4 r; for (int e = 0; e < 4; ++e) r[e] = (4 * g + e > m) ? s[e] : NEG; return r; };
  auto new_edge = [&](f32x4 s) __attribute__((always_inline)) {
    f32x4 r; for (int e = 0; e < 4; ++e) r[e] = (4 * g + e <= m) ? s[e] : NEG; return r; };
  tsf(IC<40>());
  constexpr int PK = 4;
  bf16x4 klo[18], khi[18], vr[9][4];
  sfor<PK>([&](auto ic) __attribute__((always_inline)) {
    constexpr int t = decltype(ic)::value;
    klo[t] = *(LAS bf16x4*)(ka + t * 2816); khi[t] = *(LAS bf16x4*)(kb + t * 2816);
  });
  vr[0][0] = tr_read(v0); vr[0][1] = tr_read(v0 + 2816);
  float n0 = 0.f, n1 = 0.f;
  __builtin_amdgcn_sched_barrier(0);
  if (__builtin_expect(unsafe(), 0)) {
    float mx0 = NEG, my0 = NEG, mx1 = NEG, my1 = NEG;
    sfor<18>([&](auto ic) __attribute__((always_inline)) {
      constexpr int t = decltype(ic)::value;
      const bf16x8 kf = cat(*(LAS bf16x4*)(ka + t * 2816), *(LAS bf16x4*)(kb + t * 2816));
      if constexpr (t <= 16) {
        f32x4 s = mfma(kf, qf[0], z4);
        if constexpr (t == 0) s = old_edge(s);
        if constexpr (t == 16) s = new_edge(s);
        mx0 = max3(mx0, s[0], s[1]); my0 = max3(my0, s[2], s[3]);
      }
      if constexpr (t >= 1) {
        f32x4 s = mfma(kf, qf[1], z4);
        if constexpr (t == 1) s = old_edge(s);
        if constexpr (t == 17) s = new_edge(s);
        mx1 = max3(mx1, s[0], s[1]); my1 = max3(my1, s[2], s[3]);
      }
    });
    n0 = -xmax(max3(mx0, my0, my0)); n1 = -xmax(max3(mx1, my1, my1));
  }
  tsf(IC<41>());
  const f32x4 nc0 = {n0, n0, n0, n0}, nc1 = {n1, n1, n1, n1};
  f32x4 T0[18], T1[18], O[2][2];
  unsigned pk[2][18][2];
  pk[0][17][0] = 0u; pk[0][17][1] = 0u; pk[1][0][0] = 0u; pk[1][0][1] = 0u;
  T0[0] = mfma(cat(klo[0], khi[0]), qf[0], nc0);
  __builtin_amdgcn_sched_barrier(0);
  tsf(IC<42>());
  sfor<20>([&](auto ic) __attribute__((always_inline)) {
    constexpr int t = decltype(ic)::value, st = (t - 2) >> 1;
    auto pv = [&](auto ii) __attribute__((always_inline)) {
      constexpr int i = decltype(ii)::value;
      if constexpr (t >= 2) {
        const u32x4 v = {pk[i][2 * st][0], pk[i][2 * st][1], pk[i][2 * st + 1][0], pk[i][2 * st + 1][1]};
        const bf16x8 vf = cat(vr[st][2 * (t & 1)], vr[st][2 * (t & 1) + 1]);
        if constexpr (st == 0) mfma_first(O[i][t & 1], vf, v); else if constexpr (st == 8) mfma_last(O[i][t & 1], vf, v); else mfma_more(O[i][t & 1], vf, v);
      }
    };
    if constexpr (t >= 2 && (t & 1) == 0) {
      constexpr int u = t - 1, young = (u + PK < 18 ? 2 : 0) + (u + 1 < 18 ? 2 : 0);
      __builtin_amdgcn_s_waitcnt(0xC07F | (young << 8));
    }
    float e[4];
    if constexpr (t + 1 <= 16) T0[t + 1] = mfma(cat(klo[t + 1], khi[t + 1]), qf[0], nc0);
    __builtin_amdgcn_sched_barrier(0);
    if constexpr (t + PK < 18) klo[t + PK] = *(LAS bf16x4*)(ka + (t + PK) * 2816);
    __builtin_amdgcn_sched_barrier(0);
    if constexpr (t <= 16) {
      f32x4 s = T0[t];
      if constexpr (t == 0) s = old_edge(s);
      if constexpr (t == 16) s = new_edge(s);
      for (int r = 0; r < 4; ++r) e[r] = __builtin_amdgcn_exp2f(s[r]);
    }
    __builtin_amdgcn_sched_barrier(0);
    pv(IC<0>());
    __builtin_amdgcn_sched_barrier(0);
    if constexpr ((t & 1) == 0 && t < 18) vr[t >> 1][2] = tr_read(v1 + t * 2816);
    if constexpr ((t & 1) == 1 && t + 1 < 18) vr[(t + 1) >> 1][0] = tr_read(v0 + (t + 1) * 2816);
    __builtin_amdgcn_sched_barrier(0);
    if constexpr (t <= 16) { pk[0][t][0] = pk2(e[0], e[1]); pk[0][t][1] = pk2(e[2], e[3]); }
    __builtin_amdgcn_sched_barrier(0);
    if constexpr (t + 1 <= 17) T1[t + 1] = mfma(cat(klo[t + 1], khi[t + 1]), qf[1], nc1);
    __builtin_amdgcn_sched_barrier(0);
    if constexpr (t + PK < 18) khi[t + PK] = *(LAS bf16x4*)(kb + (t + PK) * 2816);
    __builtin_amdgcn_sched_barrier(0);
    if constexpr (t >= 1 && t <= 17) {
      f32x4 s = T1[t];
      if constexpr (t == 1) s = old_edge(s);
      if constexpr (t == 17) s = new_edge(s);
      for (int r = 0; r < 4; ++r) e[r] = __builtin_amdgcn_exp2f(s[r]);
    }
    __builtin_amdgcn_sched_barrier(0);
    pv(IC<1>());
    __builtin_amdgcn_sched_barrier(0);
    if constexpr ((t & 1) == 0 && t < 18) vr[t >> 1][3] = tr_read(v1 + (t + 1) * 2816);
    if constexpr ((t & 1) == 1 && t + 1 < 18) vr[(t + 1) >> 1][1] = tr_read(v0 + (t + 2) * 2816);
    __builtin_amdgcn_sched_barrier(0);
    if constexpr (t >= 1 && t <= 17) { pk[1][t][0] = pk2(e[0], e[1]); pk[1][t][1] = pk2(e[2], e[3]); }
    __builtin_amdgcn_sched_barrier(0);
  });
  asm volatile("s_nop 7\n\ts_nop 1" : "+v"(O[0][0]), "+v"(O[0][1]), "+v"(O[1][0]), "+v"(O[1][1]));
  tsf(IC<43>());
#pragma unroll
  for (int i = 0; i < 2; ++i) {
    const float inv = __builtin_amdgcn_rcpf(bcast_row1(O[i][1][0]));
    omain[i] = pk4(O[i][0] * inv);
    oleft[i] = pk4(O[i][1] * inv);
  }
  tsf(IC<44>());
}
DI unsigned row_max(unsigned v) {
  v = __builtin_elementwise_max(v, (unsigned)__builtin_amdgcn_update_dpp(0, (int)v, 0x128, 0xf, 0xf, true));
  v = __builtin_elementwise_max(v, (unsigned)__builtin_amdgcn_update_dpp(0, (int)v, 0x124, 0xf, 0xf, true));
  v = __builtin_elementwise_max(v, (unsigned)__builtin_amdgcn_update_dpp(0, (int)v, 0x122, 0xf, 0xf, true));
  v = __builtin_elementwise_max(v, (unsigned)__builtin_amdgcn_update_dpp(0, (int)v, 0x121, 0xf, 0xf, true));
  return v;
}

#define TSCLK __builtin_amdgcn_s_memrealtime
#define TSA(k) do {} while (0)
#define TS(k) do {} while (0)
#define TSFLUSH(L) do {} while (0)

DI void store_x(const u32x2 (&pk)[5], rsrc_t rws, int lane, int odd, unsigned xb) {
  if (!odd) {
    st16(cat2(pk[0], pk[1]), rws, lane * 16u, xb);
    st16(cat2(pk[2], pk[3]), rws, lane * 16u, xb + 1024u);
    st8(pk[4], rws, lane * 16u, xb + 2048u);
  } else {
    st8(pk[0], rws, lane * 16u + 8u, xb);
    st16(cat2(pk[1], pk[2]), rws, lane * 16u, xb + 1024u);
    st16(cat2(pk[3], pk[4]), rws, lane * 16u, xb + 2048u);
  }
}

DI void poison_o(const Ctx& c, int L) {
  const u32x4 P4 = {POISON, POISON, POISON, POISON};
#pragma unroll
  for (int i = 0; i < 2; ++i) {
    const unsigned ob = WS_O + L * XF_L + (8 * c.rb + 2 * c.w + i) * 20480u;
    st16(P4, c.rws, c.lane * 16u, ob + c.j * 1024u);
    if (c.g == 0) st16(P4, c.rws, c.m * 16u, ob + (16 + (c.j >> 2)) * 1024u + (c.j & 3) * 256u);
  }
}
DI void poison_x(const Ctx& c, unsigned xs, unsigned sts) {
  const int hf = c.j & 1, cs8 = c.j >> 1, mtE = 8 * c.rb + 4 * hf + 2 * (c.w & 1) + (c.w >> 1);
  const u32x2 P2 = {POISON, POISON};
  const u32x2 pk[5] = {P2, P2, P2, P2, P2};
  store_x(pk, c.rws, c.lane, cs8 & 1, xs + mtE * 20480u + ((5 * cs8) >> 1) * 1024u);
  if (c.g == 0) st8(P2, c.rws, c.m * 8u, sts + mtE * 1024u + cs8 * 128u);
}
DI void poison_h(const Ctx& c, int L) {
  const u32x4 P4 = {POISON, POISON, POISON, POISON};
#pragma unroll
  for (int i = 0; i < 2; ++i)
#pragma unroll
    for (int u = 0; u < 3; ++u) st16(P4, c.rws, c.lane * 16u, WS_H + L * HF_L + (8 * c.rb + 2 * c.w + i) * 49152u + (3 * c.j + u) * 1024u);
}
DI void load_x(u32x4 (&r)[3], rsrc_t rws, int lane, unsigned xb) {
  const unsigned l16 = lane * 16u;
  r[0] = ld16(rws, l16, xb);
  r[1] = ld16(rws, l16, xb + 1024u);
  r[2] = ld16(rws, l16, xb + 2048u);
}
DI void split_x(const u32x4 (&r)[3], int odd, u32x2 (&x)[5]) {
  const u32x4 a = r[0], b = r[1], c = r[2];
  const unsigned a0 = a[0], a1 = a[1], a2 = a[2], a3 = a[3], b0 = b[0], b1 = b[1], b2 = b[2], b3 = b[3], c0 = c[0], c1 = c[1], c2 = c[2], c3 = c[3];
  x[0] = odd ? u32x2{a2, a3} : u32x2{a0, a1};
  x[1] = odd ? u32x2{b0, b1} : u32x2{a2, a3};
  x[2] = odd ? u32x2{b2, b3} : u32x2{b0, b1};
  x[3] = odd ? u32x2{c0, c1} : u32x2{b2, b3};
  x[4] = odd ? u32x2{c2, c3} : u32x2{c0, c1};
}

__global__ void __launch_bounds__(512, 2) fused_kernel(Params p) {
  extern __shared__ __attribute__((aligned(16))) char smem[];
  Ctx c;
  c.lds = (LAS char*)smem;
  c.lds0 = (unsigned)(size_t)c.lds;
  const int b = blockIdx.x, xcd = b & 7, loc = b >> 3;
  c.rb = 4 * (xcd >> 1) + (loc >> 3); c.j = 8 * (xcd & 1) + (loc & 7);
  c.lane = threadIdx.x & 63;
  const int wv = __builtin_amdgcn_readfirstlane(threadIdx.x >> 6);
  c.w = wv & 3;
  c.rws = mk_rsrc(p.ws, WS_END);
  c.flg = (GAS char*)p.ws + WS_FLG;
  c.tmo = (GAS unsigned*)((char*)p.ws + WS_TMO);
  c.dead = false;
  TSA(0);
  u64 zval = *(const __attribute__((address_space(4))) u64*)((const char*)p.ws + WS_Z);
  const f32x4 z4 = {0.f, 0.f, 0.f, 0.f};

  if (wv < 4) {
    {
      PHASE_VARS
      const unsigned u = 4u * b + w;
      if (u < 640u) {
        const rsrc_t rw = mk_rsrc(p.wqkv, 640u * 1920u * 2u);
        const int jj = u / 40u, s = (u % 40u) >> 1, h = u & 1u;
        const int r = lane >> 2, ch = (lane & 3) ^ (2 * ((lane >> 4) & 1));
        const unsigned stg = c.lds0 + L_PSTG + w * 4096u, sb = s * (32u * 3840u);
        const TrLane t = tr_lane(lane);
        LAS char* sl = c.lds + L_PSTG + w * 4096u;
        const unsigned dkv = WS_PQ + jj * 153600u + s * 5120u, dq = WS_PQ + jj * 153600u + L_QW + s * 2560u;
        if (h == 0) {
          const unsigned v0 = r * 3840u + qkv_col(ch, jj) * 2u, v1 = r * 3840u + qkv_col(4 + ch, jj) * 2u;
          dma16(rw, stg, v0, sb); dma16(rw, stg + 1024u, v1, sb);
          dma16(rw, stg + 2048u, v0, sb + 16u * 3840u); dma16(rw, stg + 3072u, v1, sb + 16u * 3840u);
          wait_vm0();
          const u32x4 f0 = trf_pair(sl, sl + 2048, t.la0), f1 = trf_pair(sl, sl + 2048, t.la1);
          const u32x4 f2 = trf_pair(sl + 1024, sl + 3072, t.la0), f3 = trf_pair(sl + 1024, sl + 3072, t.la1);
          st16(f0, c.rws, lane * 16u, dkv); st16(f1, c.rws, lane * 16u, dkv + 1024u);
          st16(f2, c.rws, lane * 16u, dkv + 2048u); st16(f3, c.rws, lane * 16u, dkv + 3072u);
          __builtin_amdgcn_raw_buffer_store_b32(sel4(g, tile_colsum(f0), tile_colsum(f1), tile_colsum(f2), tile_colsum(f3)), c.rws, lane * 4u, WS_CPQ + (jj * 20 + s) * 512u, 16);
        } else {
          const unsigned v2 = r * 3840u + qkv_col(8 + ch, jj) * 2u;
          const unsigned vs = (lane >> 1) * 3840u + qkv_col(12 + (lane & 1), jj) * 2u;
          const unsigned vh = (lane & 31) * 3840u + qkv_col(14, jj) * 2u;
          dma16(rw, stg, v2, sb); dma16(rw, stg + 1024u, v2, sb + 16u * 3840u);
          dma16(rw, stg + 2048u, vs, sb); dma16(rw, stg + 3072u, vh, sb);
          wait_vm0();
          const u32x4 f4 = trf_pair(sl, sl + 1024, t.la0), f5 = trf_pair(sl, sl + 1024, t.la1);
          const u32x4 f6 = trf_pair(sl + 2048, sl + 2560, t.ls), f7 = trf_pair(sl + 3072, sl + 3328, t.lh);
          st16(f4, c.rws, lane * 16u, dkv + 4096u); st16(f5, c.rws, lane * 16u, dq);
          st16(f6, c.rws, lane * 16u, dq + 1024u);
          if (m < 8) st16(f7, c.rws, g * 128u + m * 16u, dq + 2048u);
          __builtin_amdgcn_raw_buffer_store_b32(sel4(g, tile_colsum(f4), tile_colsum(f5), tile_colsum(f6), tile_colsum(f7)), c.rws, lane * 4u, WS_CPQ + (jj * 20 + s) * 512u + 256u, 16);
        }
      }
      asm volatile("" : "+s"(zval));
      c.tag = zval * 0x9E3779B97F4A7C15ull + 0x632BE59BD9B4E019ull;
      if (u < 640u) publish(c, F_PQ(u));
      TSA(1);
      nap<PZ_NAP>();
      poison_o(c, 0); poison_x(c, WS_X1, WS_ST1);
    }

#pragma unroll 1
    for (int L = 0; L < 2; ++L) {
      {
        PHASE_VARS
        TS(0);
        Junk jk;
        u32x4 a[2][20], st[2];
        const unsigned stb = WS_STI + L * ST_L + (8 * rb + 2 * w) * 1024u;
        if (L == 0) { if (4 * (int)blockIdx.x + w < 640) nap<X0_PRE>(); else nap<X0_PRE2>(); wait_vm0(); wait_flags(c, F_X0((8 * rb + 2 * w) * 8), 16, 1); }
        jk = Junk{0, 0, 0};
        if (L == 0) wait_stats<0>(c, st, stb, 2); else wait_stats<DPRE_Q>(c, st, stb, 2);
        TS(1);
        constexpr int WA = 10;
        const unsigned xb = WS_XI + L * XF_L + (8 * rb + 2 * w) * 20480u;
        const unsigned l16 = lane * 16u;
        auto la = [&](int i, int s) __attribute__((always_inline)) { return ld16(c.rws, l16 + (s & 3) * 1024u, xb + i * 20480u + (s >> 2) * 4096u); };
#pragma unroll
        for (int s = 0; s < WA; ++s) { a[0][s] = la(0, s); a[1][s] = la(1, s); }
        __builtin_amdgcn_sched_barrier(0);
        float mean[2], rstd[2];
        row_stats2(st, mean, rstd);
        TS(2);
        bar_lgkm();
        TS(3);
        hold(jk);
        const unsigned kvg = WS_KV + L * KV_L + (rb * 16 + j) * KV_WG;
        {
          f32x4 acc[2][5];
          LAS char* f0 = opaque(c.lds + l16); LAS char* f1 = opaque(c.lds + l16 + 61440);
          gemm_steps<20, 5, WA, (DP_X2 && INLOOP)>(c, acc, a,
            [&](int s, int t) __attribute__((always_inline)) { return lds_frag((s < 12 ? f0 + s * 5120 : f1 + (s - 12) * 5120) + t * 1024); },
            la, [&](auto ic) __attribute__((always_inline)) {
              if constexpr (decltype(ic)::value == 5 * ((4 * EQ_FIRST) / 5) - PD) if (L == 0) bar_raw();
            });
          if (__builtin_expect(has_nan(acc[0][0], acc[1][0]), 0)) {
            redo_gemm<20, 5>(c, acc, la, WS_PQ + j * 153600u);
#pragma unroll
            for (int s = 0; s < 20; ++s) { a[0][s] = la(0, s); a[1][s] = la(1, s); }
          }
          TS(4);
          bar_raw();
          TS(5);
          refresh(c);
          const int m = c.m, g = c.g;
          const unsigned l16 = c.lane * 16u;
          const unsigned vb = 640u + (m & 8) * 48u + (m & 7) * 8u, kb = m * 8u;
          LAS char* ld0 = c.lds + (16 + 2 * w) * 2816;
          float nn[2] = {0.f, 0.f};
#pragma unroll
          for (int t = 0; t < 5; ++t) {
            const f32x4 cs = *(LAS f32x4*)(c.lds + L_CSQ + (16 * t + 4 * g) * 4);
            unsigned ko;
            if (t == 0) ko = kb + g * 128u;
            else if (t == 1) ko = g == 0 ? kb + 512u : 1408u + kb + (g - 1) * 128u;
            else if (t == 2) ko = g < 2 ? 1408u + kb + (3 + g) * 128u : vb + (g - 2) * 64u;
            else if (t == 3) ko = g < 3 ? vb + (2 + g) * 64u : 1408u + vb;
            else ko = 1408u + vb + (1 + g) * 64u;
#pragma unroll
            for (int i = 0; i < 2; ++i) {
              const u32x2 kv = pk4((acc[i][t] - mean[i] * cs) * rstd[i]);
              *(LAS u32x2*)(ld0 + i * 2816 + ko) = kv;
              const unsigned k0 = kv[0], k1 = kv[1];
              nn[i] = dot2(k0, k0, dot2(k1, k1, nn[i]));
            }
          }
          if (g < 2) {
            const u32x2 one = {0x00003f80u, 0u};
            *(LAS u32x2*)(ld0 + g * 1408 + vb + 320) = one;
            *(LAS u32x2*)(ld0 + 2816 + g * 1408 + vb + 320) = one;
          }
#pragma unroll
          for (int e = 1; e < 5; ++e) st16(*(LAS u32x4*)(ld0 + e * 1024 + l16), c.rws, l16, kvg + w * 5632u + e * 1024u);
          if (c.lane < 32) st16(*(LAS u32x4*)(ld0 + 5120 + l16), c.rws, l16, kvg + w * 5632u + 5120u);
          __builtin_amdgcn_sched_barrier(0);
          const unsigned nb = row_max(__builtin_elementwise_max(__builtin_bit_cast(unsigned, xsum(nn[0])), __builtin_bit_cast(unsigned, xsum(nn[1]))));
          if (c.lane == 0) *(LAS unsigned*)(ld0 + 964) = __builtin_elementwise_min((nb >> 16) + 1u, 0x7f7fu);
          st16(*(LAS u32x4*)(ld0 + l16), c.rws, l16, kvg + w * 5632u);
          __builtin_amdgcn_sched_barrier(0);
          TS(6);
          bar_raw();
          TS(8);
        }
        bf16x8 qf[2][2];
        float qn = 0.f;
        {
          refresh(c);
          const int m = c.m, g = c.g;
          const unsigned l16 = c.lane * 16u;
          f32x4 acc[2][3];
          LAS char* f0 = opaque(c.lds + l16 + L_QW);
          LAS char* h0 = opaque(c.lds + L_QW + 2048 + g * 128u + (m & 7) * 16u);
          gemm_steps<20, 3, 20, false>(c, acc, a,
            [&](int s, int t) __attribute__((always_inline)) { return lds_frag(t < 2 ? f0 + s * 2560 + t * 1024 : h0 + s * 2560); },
            la,
            [&](auto ic) __attribute__((always_inline)) {
              constexpr int I = decltype(ic)::value;
              if constexpr (I == KVPUB_AT) publish(c, F_KV(L * 1024 + (rb * 16 + j) * 4 + w));
            });
          TS(7);
#pragma unroll
          for (int i = 0; i < 2; ++i) {
            u32x2 pk[3];
            float rq = rstd[i];
            asm volatile("" : "+v"(rq));
            rq *= 0.22360679774997896f * 1.4426950408889634f;
#pragma unroll
            for (int t = 0; t < 3; ++t) {
              const f32x4 cs = *(LAS f32x4*)(c.lds + L_CSQ + (16 * (5 + t) + 4 * g) * 4);
              pk[t] = pk4((acc[i][t] - mean[i] * cs) * rq);
            }
            const u32x2 zz = {0u, 0u};
            const u32x2 zm = {g == 3 ? 0x3f800000u : 0u, 0u};
            {
              const unsigned a0 = pk[0][0], a1 = pk[0][1], b0 = pk[1][0], b1 = pk[1][1], c0 = pk[2][0], c1 = pk[2][1];
              const float q01 = dot2(a0, a0, dot2(a1, a1, dot2(b0, b0, dot2(b1, b1, 0.f)))), q2 = dot2(c0, c0, dot2(c1, c1, 0.f));
              qn = __builtin_fmaxf(qn, xsum(q01 + (g < 2 ? q2 : 0.f)));
            }
            qf[0][i] = asbf(cat2(pk[0], g == 0 ? pk[1] : zm));
            qf[1][i] = asbf(cat2(g == 0 ? zz : pk[1], g < 2 ? pk[2] : zm));
          }
        }
        refresh(c);
        AttnAddr ad = attn_addr(c, 0), ad1 = attn_addr(c, 1);
        ad.ka = opaque(ad.ka); ad.kb = opaque(ad.kb); ad.v0 = opaque(ad.v0); ad.v1 = opaque(ad.v1);
        ad1.ka = opaque(ad1.ka); ad1.kb = opaque(ad1.kb); ad1.v0 = opaque(ad1.v0); ad1.v1 = opaque(ad1.v1);
        LAS char* kwp = opaque(c.lds + (2 * c.w + 2 * (c.m < 8 ? c.m : 8)) * 2816 + 964);
        asm volatile("" : "+v"(qn), "+v"(qf[0][0]), "+v"(qf[0][1]), "+v"(qf[1][0]), "+v"(qf[1][1]));
        TS(9);
        bar_lgkm();
        TS(10);
        const unsigned kw = *(LAS unsigned*)kwp;
        bool exact = false;
        u32x2 om[2][2], ol[2][2];
        om[0][0] = om[0][1] = ol[0][0] = ol[0][1] = u32x2{0u, 0u};
#pragma unroll 1
        for (int h = 0; h < 2; ++h) {
          bf16x8 q2[2] = {h ? qf[1][0] : qf[0][0], h ? qf[1][1] : qf[0][1]};
          attn_head2([&](auto ic) __attribute__((always_inline)) { if (h == DBG_H) TS(decltype(ic)::value); },
                     [&]() __attribute__((always_inline)) {
                       if (h == 0) {
                         const float kn = __builtin_bit_cast(float, row_max(kw) << 16);
                         exact = __builtin_amdgcn_ballot_w64(!(qn * kn <= 6400.0f && kn <= 4294967296.0f)) != 0 || FORCE_EXACT;
                       }
                       return exact;
                     }, c, ad, q2, om[1], ol[1]);
          if (h == 0) {
            om[0][0] = om[1][0]; om[0][1] = om[1][1]; ol[0][0] = ol[1][0]; ol[0][1] = ol[1][1];
            ad = ad1;
          }
        }
        TS(11);
        __builtin_amdgcn_s_waitcnt(0x0F70);
        refresh(c);
#pragma unroll
        for (int i = 0; i < 2; ++i) {
          const int m = c.m, g = c.g;
          const unsigned l16 = c.lane * 16u;
          const unsigned mt = 8 * rb + 2 * w + i, ob = WS_O + L * XF_L + mt * 20480u;
          st16(san4(cat2(om[0][i], om[1][i])), c.rws, l16, ob + j * 1024u);
          if (g == 0) st16(san4(cat2(ol[0][i], ol[1][i])), c.rws, m * 16u, ob + (16 + (j >> 2)) * 1024u + (j & 3) * 256u);
        }
        TS(12);
        bar_raw();
        TS(13);
      }
      {
        PHASE_VARS
        u32x4 a[2][10];
        Junk jk;
        const unsigned ob = WS_O + L * XF_L + (8 * rb + 4 * hf + 2 * rg) * 20480u;
        jk = Junk{0, 0, 0};
        wait_probe<DPRE_O>(c, (lane & 7) * 256u + 252u, ob + 20480u + (16 + 2 * kh) * 1024u, 4);
        TS(14);
        const unsigned l16 = lane * 16u;
        auto la = [&](int i, int s) __attribute__((always_inline)) {
          return ld16(c.rws, l16 + (s < 8 ? (s & 3) : (s - 8)) * 1024u, ob + i * 20480u + (s < 8 ? (8 * kh + (s & 4)) : 16 + 2 * kh) * 1024u); };
#pragma unroll
        for (int s = 0; s < 10; ++s) { a[0][s] = la(0, s); a[1][s] = la(1, s); }
        u32x4 xr3[3];
        load_x(xr3, c.rws, lane, WS_XI + L * XF_L + mtE * 20480u + ((5 * cs8) >> 1) * 1024u);
        __builtin_amdgcn_sched_barrier(0);
        TS(15);
        bar_raw();
        TS(16);
        f32x4 acc[2][5];
        {
          LAS char* fa = opaque(c.lds + l16 + L_WOA + kh * 40960); LAS char* fb = opaque(c.lds + l16 + kh * 10240);
          gemm_steps<10, 5, 10, (DP_O && INLOOP)>(c, acc, a,
            [&](int s, int t) __attribute__((always_inline)) { const int q = 5 * s + t; return lds_frag(q < 40 ? fa + q * 1024 : fb + (q - 40) * 1024); },
            la, [&](auto ic) __attribute__((always_inline)) { if constexpr (decltype(ic)::value == 1) hold(jk); });
        }
        if (__builtin_expect(has_nan(acc[0][0], acc[1][0]), 0)) redo_gemm<10, 5>(c, acc, la, WS_PO + cs8 * 102400u + kh * 51200u);
        TS(17);
        LAS char* red = c.lds + L_REDO + l16;
#pragma unroll
        for (int t = 0; t < 5; ++t) *(LAS f32x4*)(red + w * 5120 + t * 1024) = kh ? acc[0][t] : acc[1][t];
        bar_lgkm();
        TS(18);
        float sum = 0.f, sq = 0.f;
        u32x2 pk[5], xres[5];
        split_x(xr3, cs8 & 1, xres);
#pragma unroll
        for (int t = 0; t < 5; ++t) {
          const f32x4 o = *(LAS f32x4*)(red + (w ^ 2) * 5120 + t * 1024);
          const f32x4 mine = kh ? acc[1][t] : acc[0][t];
          pk[t] = san2(pk4(unpk4(xres[t]) + (mine + o)));
          acc_stats(pk[t], sum, sq);
        }
        store_x(pk, c.rws, lane, cs8 & 1, WS_X1 + L * XF_L + mtE * 20480u + ((5 * cs8) >> 1) * 1024u);
        sum = xsum(sum); sq = xsum(sq);
        if (g == 0) { u32x2 v = {san(__builtin_bit_cast(unsigned, sum)), san(__builtin_bit_cast(unsigned, sq))}; st8(v, c.rws, m * 8u, WS_ST1 + L * ST_L + mtE * 1024u + cs8 * 128u); }
        TS(19);
        TS(20);
      }
      {
        PHASE_VARS
        Junk jk;
        u32x4 a[2][20], st[2];
        const unsigned stb = WS_ST1 + L * ST_L + (8 * rb + 2 * w) * 1024u;
        jk = Junk{0, 0, 0};
        wait_stats<DPRE_W1>(c, st, stb, 5);
        TS(21);
        constexpr int WA = WA_W1;
        const unsigned xb = WS_X1 + L * XF_L + (8 * rb + 2 * w) * 20480u;
        const unsigned l16 = lane * 16u;
        auto la = [&](int i, int s) __attribute__((always_inline)) { return ld16(c.rws, l16 + (s & 3) * 1024u, xb + i * 20480u + (s >> 2) * 4096u); };
#pragma unroll
        for (int s = 0; s < WA; ++s) { a[0][s] = la(0, s); a[1][s] = la(1, s); }
        __builtin_amdgcn_sched_barrier(0);
        float mean[2], rstd[2];
        row_stats2(st, mean, rstd);
        TS(22);
        bar_raw();
        TS(23);
        hold(jk);
        f32x4 acc[2][6];
        {
          LAS char* f0 = opaque(c.lds + l16 + L_W1A); LAS char* f1 = opaque(c.lds + l16 + L_W1A + 61440); LAS char* f2 = opaque(c.lds + l16);
          gemm_steps<20, 6, WA, (DP_X1 && INLOOP)>(c, acc, a,
            [&](int s, int t) __attribute__((always_inline)) {
              const int q = 6 * s + t;
              return lds_frag(q < 60 ? f0 + q * 1024 : (q < 108 ? f1 + (q - 60) * 1024 : f2 + (q - 108) * 1024)); },
            la, [&](auto ic) __attribute__((always_inline)) { if constexpr (decltype(ic)::value == W1_BAR) bar_raw(); });
        }
        if (__builtin_expect(has_nan(acc[0][0], acc[1][0]), 0)) redo_gemm<20, 6>(c, acc, la, WS_P1 + j * 122880u);
        TS(24);
        bar_raw();
        TS(25);
#pragma unroll
        for (int i = 0; i < 2; ++i) {
          u32x2 pk[6];
#pragma unroll
          for (int t = 0; t < 6; ++t) {
            const f32x4 cs = *(LAS f32x4*)(c.lds + L_CS1 + (16 * t + 4 * g) * 4);
            const f32x4 bb = *(LAS f32x4*)(c.lds + L_B1 + (16 * t + 4 * g) * 4);
            f32x4 v = (acc[i][t] - mean[i] * cs) * rstd[i] + bb;
#pragma unroll
            for (int r = 0; r < 4; ++r) { const float x = fmaxf(v[r], 0.f); v[r] = x * x; }
            pk[t] = pk4(v);
          }
          const unsigned hb = WS_H + L * HF_L + (8 * rb + 2 * w + i) * 49152u + 3 * j * 1024u;
#pragma unroll
          for (int u = 0; u < 3; ++u) st16(cat2(pk[2 * u], pk[2 * u + 1]), c.rws, l16, hb + u * 1024u);
        }
        TS(26);
        TS(27);
      }
      {
        PHASE_VARS
        Junk jk;
        const unsigned hb = WS_H + L * HF_L + (8 * rb + 4 * hf + 2 * rg) * 49152u + 24 * kh * 1024u;
        jk = Junk{0, 0, 0};
        wait_probe<DPRE_W2>(c, (lane & 7) * 3072u + 3068u, hb + 49152u, 6);
        TS(28);
        constexpr int WA = WA_W2;
        u32x4 a[2][24];
        const unsigned l16 = lane * 16u;
        auto la = [&](int i, int s) __attribute__((always_inline)) { return ld16(c.rws, l16 + (s & 3) * 1024u, hb + i * 49152u + (s >> 2) * 4096u); };
#pragma unroll
        for (int s = 0; s < WA; ++s) { a[0][s] = la(0, s); a[1][s] = la(1, s); }
        u32x4 xr3[3];
        load_x(xr3, c.rws, lane, WS_X1 + L * XF_L + mtE * 20480u + ((5 * cs8) >> 1) * 1024u);
        __builtin_amdgcn_sched_barrier(0);
        TS(29);
        bar_raw();
        TS(30);
        f32x4 acc[2][5];
        {
          LAS char* f0 = opaque(c.lds + l16 + kh * 76800); LAS char* f1 = opaque(c.lds + l16 + kh * 76800 + 40960);
          gemm_steps<24, 5, WA, (DP_H && INLOOP)>(c, acc, a,
            [&](int s, int t) __attribute__((always_inline)) {
              const int sl = s < 15 ? s : s - 15;
              return lds_frag(sl < 8 ? f0 + sl * 5120 + t * 1024 : f1 + (sl - 8) * 5120 + t * 1024); },
            la,
            [&](auto ic) __attribute__((always_inline)) {
              constexpr int I = decltype(ic)::value;
              if constexpr (I == 1) hold(jk);
              if constexpr (I == 25 || I == 50 || I == 100 - PD) bar_raw();
            });
        }
        if (__builtin_expect(has_nan(acc[0][0], acc[1][0]), 0)) redo_gemm<24, 5>(c, acc, la, WS_P2 + cs8 * 245760u + kh * 122880u);
        TS(31);
        LAS char* red = c.lds + L_RED2 + l16;
#pragma unroll
        for (int t = 0; t < 5; ++t) *(LAS f32x4*)(red + w * 5120 + t * 1024) = kh ? acc[0][t] : acc[1][t];
        bar_lgkm();
        TS(32);
        float sum = 0.f, sq = 0.f;
        u32x2 pk[5], xres[5];
        split_x(xr3, cs8 & 1, xres);
        f32x4 op[5];
#pragma unroll
        for (int t = 0; t < 5; ++t) op[t] = *(LAS f32x4*)(red + (w ^ 2) * 5120 + t * 1024);
        if (L == 0) bar_lgkm();
#pragma unroll
        for (int t = 0; t < 5; ++t) {
          const f32x4 o = op[t];
          const f32x4 bb = *(LAS f32x4*)(c.lds + L_B2 + (16 * t + 4 * g) * 4);
          const f32x4 mine = kh ? acc[1][t] : acc[0][t];
          pk[t] = san2(pk4(unpk4(xres[t]) + ((mine + o) + bb)));
          acc_stats(pk[t], sum, sq);
        }
        if (L == 0) {
          store_x(pk, c.rws, lane, cs8 & 1, WS_XI + XF_L + mtE * 20480u + ((5 * cs8) >> 1) * 1024u);
          sum = xsum(sum); sq = xsum(sq);
          if (g == 0) { u32x2 v = {san(__builtin_bit_cast(unsigned, sum)), san(__builtin_bit_cast(unsigned, sq))}; st8(v, c.rws, m * 8u, WS_STI + ST_L + mtE * 1024u + cs8 * 128u); }
          TS(33);
          TS(34);
        } else {
          const rsrc_t rout = mk_rsrc(p.out, 2048u * 640u * 2u);
          const unsigned vo = (mtE * 16 + m) * 1280u + cs8 * 160u + g * 8u;
#pragma unroll
          for (int t = 0; t < 5; ++t) __builtin_amdgcn_raw_buffer_store_b64(pk[t], rout, vo, t * 32u, OUT_AUX);
        }
      }
      TS(39);
      TSFLUSH(L);
    }
  } else {
    const int d = c.w;
    u32x4 ru[6];
    float cq[10];
    unsigned udst = 0;
    const unsigned v = 4u * b + d;
    auto retile = [&]() __attribute__((always_inline)) {
      const int lane = c.lane, g = c.g;
      const unsigned l16 = lane * 16u;
          const TrLane t = tr_lane(lane);
          LAS char* sl = c.lds + L_STG + d * 2048u;
          const bool is1 = v >= 160u && v < 480u;
          float cs[4];
#pragma unroll
          for (int e = 0; e < 2; ++e) {
            lds16(sl + l16, ru[2 * e]); lds16(sl + 1024 + l16, ru[2 * e + 1]);
            const u32x4 f0 = trf_pair(sl, sl + 1024, t.la0), f1 = trf_pair(sl, sl + 1024, t.la1);
            st16(f0, c.rws, l16, udst + e * 2048u); st16(f1, c.rws, l16, udst + e * 2048u + 1024u);
            if (is1) { cs[2 * e] = tile_colsum(f0); cs[2 * e + 1] = tile_colsum(f1); }
          }
          lds16(sl + l16, ru[4]);
          if (is1) {
            lds16(sl + 1024 + l16, ru[5]);
            const u32x4 f0 = trf_pair(sl, sl + 1024, t.la0), f1 = trf_pair(sl, sl + 1024, t.la1);
            st16(f0, c.rws, l16, udst + 4096u); st16(f1, c.rws, l16, udst + 5120u);
            const unsigned cb = WS_CP1 + (v - 160u) * 384u;
            __builtin_amdgcn_raw_buffer_store_b32(sel4(g, cs[0], cs[1], cs[2], cs[3]), c.rws, lane * 4u, cb, 16);
            const float c4 = tile_colsum(f0), c5 = tile_colsum(f1);
            if (g < 2) __builtin_amdgcn_raw_buffer_store_b32(sel4(g, c4, c5, c5, c5), c.rws, lane * 4u, cb + 256u, 16);
          } else {
            st16(trf_pair(sl, sl + 512, t.ls), c.rws, l16, udst + 4096u);
          }
    };
    {
      PHASE_VARS
      auto unit_loads = [&]() __attribute__((always_inline)) {
      const int r = lane >> 2, ch = (lane & 3) ^ (2 * ((lane >> 4) & 1));
      const bool is1 = v >= 160u && v < 480u, wo = v < 160u;
      const rsrc_t rw = mk_rsrc(is1 ? p.w1 : (wo ? p.wo : p.w2), is1 ? 640u * 1536u * 2u : (wo ? 640u * 640u * 2u : 1536u * 640u * 2u));
      unsigned vp, vs, sb, tadv;
      if (is1) {
        const unsigned e = v - 160u, jj = e / 20u, s = e % 20u;
        vp = r * 3072u + jj * 192u + ch * 16u; vs = vp; sb = s * (32u * 3072u); tadv = 16u * 3072u;
        udst = WS_P1 + jj * 122880u + s * 6144u;
      } else {
        const unsigned e = wo ? v : (v < 864u ? v - 480u : 0u), c8 = wo ? e / 20u : e / 48u, s = wo ? e % 20u : e % 48u;
        const unsigned cb = c8 * 160u;
        const int rho = lane >> 1, r2 = rho & 15;
        if (!wo) {
          vp = r * 1280u + cb + ch * 16u; vs = rho * 1280u + cb + 128u + (lane & 1) * 16u;
          sb = s * (32u * 1280u); tadv = 16u * 1280u; udst = WS_P2 + c8 * 245760u + s * 5120u;
        } else if (s < 16u) {
          vp = r * 1280u + cb + ch * 16u; vs = (20 * (rho >> 4) + r2) * 1280u + cb + 128u + (lane & 1) * 16u;
          sb = s * (40u * 1280u); tadv = 20u * 1280u; udst = WS_PO + c8 * 102400u + (s >> 3) * 51200u + (s & 7u) * 5120u;
        } else {
          vp = (40 * (r >> 2) + (r & 3)) * 1280u + cb + ch * 16u;
          vs = (40 * (r2 >> 2) + 20 * (rho >> 4) + (r2 & 3)) * 1280u + cb + 128u + (lane & 1) * 16u;
          sb = (160u * (s - 16u) + 16u) * 1280u; tadv = 20u * 1280u;
          udst = WS_PO + c8 * 102400u + ((s - 16u) >> 1) * 51200u + (8u + (s & 1u)) * 5120u;
        }
      }
      sb = __builtin_amdgcn_readfirstlane(sb); tadv = __builtin_amdgcn_readfirstlane(tadv); udst = __builtin_amdgcn_readfirstlane(udst);
      ru[0] = ld16p(rw, vp, sb); ru[1] = ld16p(rw, vp, sb + tadv); ru[2] = ld16p(rw, vp, sb + 64u); ru[3] = ld16p(rw, vp, sb + tadv + 64u);
      ru[4] = ld16p(rw, is1 ? vp + 128u : vs, sb); ru[5] = ld16p(rw, vp + 128u, sb + tadv);
      };
      nap<X_NAP>();
      {
        const rsrc_t rx = mk_rsrc(p.x, 2048u * 640u * 2u);
        const unsigned mt = 8 * rb + 2 * d + (j & 1), sl = j >> 1, s0 = sl < 4 ? 3 * sl : 4 + 2 * sl;
        u32x2 xl[3][2];
#pragma unroll
        for (int s = 0; s < 3; ++s) {
          const unsigned so = mt * (16u * 1280u) + (s0 + (s < 2 || sl < 4 ? s : 0)) * 64u;
          xl[s][0] = ld8p(rx, m * 1280u + g * 8u, so);
          xl[s][1] = ld8p(rx, m * 1280u + g * 8u + 32u, so);
        }
        unsigned bb = 0;
        if (d == 0) bb = __builtin_amdgcn_raw_buffer_load_b32(mk_rsrc(p.b1, 1536u * 2u), lane * 4u, j * 192u, 0);
        if (d == 1) bb = __builtin_amdgcn_raw_buffer_load_b32(mk_rsrc(p.b2, 640u * 2u), lane * 4u, cs8 * 160u, 0);
        pin();
        float s1 = 0.f, q1 = 0.f;
#pragma unroll
        for (int s = 0; s < 3; ++s)
          if (s < 2 || sl < 4) {
            const u32x4 f = san4(cat2(xl[s][0], xl[s][1]));
            st16(f, c.rws, lane * 16u, WS_XI + mt * 20480u + (s0 + s) * 1024u);
            const unsigned f0 = f[0], f1 = f[1], f2 = f[2], f3 = f[3];
            s1 = dot2(f0, 0x3f803f80u, s1); q1 = dot2(f0, f0, q1); s1 = dot2(f1, 0x3f803f80u, s1); q1 = dot2(f1, f1, q1);
            s1 = dot2(f2, 0x3f803f80u, s1); q1 = dot2(f2, f2, q1); s1 = dot2(f3, 0x3f803f80u, s1); q1 = dot2(f3, f3, q1);
          }
        s1 = xsum(s1); q1 = xsum(q1);
        const u32x2 sv = {san(__builtin_bit_cast(unsigned, s1)), san(__builtin_bit_cast(unsigned, q1))};
        if (g == 0) st8(sv, c.rws, m * 8u, WS_STI + mt * 1024u + sl * 128u);
        asm volatile("" : "+s"(zval));
        c.tag = zval * 0x9E3779B97F4A7C15ull + 0x632BE59BD9B4E019ull;
        publish(c, F_X0(mt * 8 + sl));
        const f32x2 o = {bflo(bb), bfhi(bb)};
        if (d == 0 && lane < 48) *(LAS f32x2*)(c.lds + L_B1 + lane * 8) = o;
        if (d == 1 && lane < 40) *(LAS f32x2*)(c.lds + L_B2 + lane * 8) = o;
        if (d == 2 && lane == 0) *(LAS unsigned*)(c.lds + L_CNT) = 0u;
      }
      TSA(1);
      unit_loads();
      pin();
      nap<PQ_PRE>();
      wait_vm0();
      wait_flags(c, F_PQ(j * 40), 40, 7);
      TSA(2);
      gather_load_raw(cq, c.rws, WS_CPQ + j * (20u * 512u) + d * 128u, 512u, lane);
      pin();
      for (int q = d; q < 4 * EQ_ISSUE; q += 4) dma16(c.rws, c.lds0 + q * 1024u, lane * 16u, WS_PQ + j * 153600u + q * 1024u);
      TSA(3);
      pin();
      TSA(4);
      asm volatile("s_waitcnt vmcnt(%0)" :: "n"((RU_AT == 0 ? 6 : 0) + EQ_ISSUE - EQ_FIRST) : "memory");
      TSA(5);
    }
    auto layer = [&](auto lc) __attribute__((always_inline)) {
      constexpr int L = decltype(lc)::value;
      PHASE_VARS
      const unsigned l16 = lane * 16u, voff = l16 + d * 1024u;
      LAS char* lw = c.lds + voff;
      TS(3);
      bar_lgkm();
      if constexpr (L == 0) {
        TS(4);
        for (int q = 4 * EQ_ISSUE + d; q < 150; q += 4) dma16(c.rws, c.lds0 + q * 1024u, l16, WS_PQ + j * 153600u + q * 1024u);
        asm volatile("s_waitcnt vmcnt(12)" ::: "memory");
        landed(cq);
        TS(2);
        {
          const float cs = gather_sum(cq);
          if (lane < 32) *(LAS float*)(c.lds + L_CSQ + (32 * d + lane) * 4) = cs;
        }
        pin();
        bar_raw();
        TS(6);
        if (d == 0 && lane == 0)
          __hip_atomic_store((GAS u64*)(c.flg - WS_FLG + WS_ARR) + blockIdx.x, c.tag, __ATOMIC_RELAXED, __HIP_MEMORY_SCOPE_AGENT);
        if (v < 864u) retile();
        TS(1);
        asm volatile("s_waitcnt lgkmcnt(0)" ::: "memory");
        TS(5);
      }
      bar_raw();
      if constexpr (L == 0) {
        if (v >= 864u) wait_vm0();
        else if (v >= 160u && v < 480u) asm volatile("s_waitcnt vmcnt(8)" ::: "memory");
        else asm volatile("s_waitcnt vmcnt(5)" ::: "memory");
      }
      TS(7);
      bar_raw();
      if constexpr (L == 0) if (v < 864u) publish(c, F_P2(v));
      u32x4 ro[2][3], r1[30], r2[2][30];
      u64 pt0 = 0, pt1 = 0;
      if constexpr (L == 1 && R1_EARLY == 1) {
#pragma unroll
        for (int k = 0; k < 30; ++k) r1[k] = ld16(c.rws, voff, WS_P1 + j * 122880u + k * 4096u);
#pragma unroll
        for (int h = 0; h < 2; ++h)
#pragma unroll
          for (int k = 0; k < 3; ++k) ro[h][k] = ld16(c.rws, voff, WS_PO + cs8 * 102400u + h * 51200u + (10 + k) * 4096u);
        pin();
      }
      {
        Junk jk = {0, 0, 0};
        if (L == 0 || rb >= 1) {
          GAS u64* fp = (GAS u64*)(c.flg + F_KV(L * 1024 + ((rb - 1 - (rb >= 2 ? (lane & 1) : 0)) * 16 + j) * 4 + d).off);
          if (L == 0 && (lane >= 32 || rb == 0)) fp = (GAS u64*)(c.flg - WS_FLG + WS_ARR) + blk_of(rb, lane & 15);
          if (L == 0 && MID_FIRST == 3) nap<HALO_SLEEP0>(); else nap<HALO_SLEEP>();
          poll_fast(c, fp, 3u, jk);
        }
        TS(8);
#pragma unroll
        for (int e = 0; e < 2; ++e) {
          const int srb = rb - 2 + e;
          const unsigned la = e * KV_WG + d * 5632u;
          if (srb >= 0) {
            const unsigned sb = WS_KV + L * KV_L + (srb * 16 + j) * KV_WG + d * 5632u;
#pragma unroll
            for (int i = 0; i < 5; ++i) dma16(c.rws, c.lds0 + la + i * 1024u, l16, sb + i * 1024u);
            if (lane < 32) dma16(c.rws, c.lds0 + la + 5120u, l16, sb + 5120u);
          } else {
            const u32x4 z = {0u, 0u, 0u, 0u};
#pragma unroll
            for (int i = 0; i < 5; ++i) lds16(c.lds + la + i * 1024u + l16, z);
            if (lane < 32) lds16(c.lds + la + 5120u + l16, z);
            *(LAS u32x2*)(c.lds + la + (lane >> 4) * 1408u + 960u + ((lane >> 3) & 1) * 384u + (lane & 7) * 8u) = u32x2{0xf1490000u, 0u};
          }
        }
        if constexpr (L == 0 && P2_PEEK) {
          pin();
          const unsigned i0 = lane < 20 ? cs8 * 20 + lane : (lane < 40 ? 160 + j * 20 + (lane - 20) : cs8 * 20);
          const unsigned i1 = 480 + cs8 * 48 + (lane < 48 ? lane : 0);
          pt0 = ld_flag((GAS u64*)(c.flg + FB_P2 + i0 * S_PRE)); pt1 = ld_flag((GAS u64*)(c.flg + FB_P2 + i1 * S_PRE));
          pin();
          asm volatile("s_waitcnt vmcnt(2) lgkmcnt(0)" ::: "memory");
        } else
        if constexpr (L == 1 && R1_EARLY == 2) {
          pin();
#pragma unroll
          for (int h = 0; h < 2; ++h)
#pragma unroll
            for (int k = 0; k < 3; ++k) ro[h][k] = ld16(c.rws, voff, WS_PO + cs8 * 102400u + h * 51200u + (10 + k) * 4096u);
#pragma unroll
          for (int k = 0; k < R1N; ++k) r1[k] = ld16(c.rws, voff, WS_P1 + j * 122880u + k * 4096u);
          pin();
          asm volatile("s_waitcnt vmcnt(%0) lgkmcnt(0)" :: "n"(6 + R1N) : "memory");
        } else {
          asm volatile("s_waitcnt vmcnt(0) lgkmcnt(0)" ::: "memory");
        }
        hold(jk);
        TS(9);
      }
      bar_raw();
      bool p2ok = false;
      if constexpr (L == 0 && P2_PEEK) {
        asm volatile("" : "+v"(pt0), "+v"(pt1));
        p2ok = __all(pt0 == c.tag && pt1 == c.tag);
      }
      if constexpr (L == 0) if (!p2ok) wait_flags(c, F_P2(cs8 * 20), 20, 8);
#pragma unroll
      for (int h = 0; h < 2; ++h)
#pragma unroll
        for (int k = 0; k < 10; ++k) dma16(c.rws, c.lds0 + L_WOA + h * 40960 + k * 4096 + d * 1024u, voff, WS_PO + cs8 * 102400u + h * 51200u + k * 4096u);
      if constexpr (L == 0 || !R1_EARLY) {
#pragma unroll
        for (int h = 0; h < 2; ++h)
#pragma unroll
          for (int k = 0; k < 3; ++k) ro[h][k] = ld16(c.rws, voff, WS_PO + cs8 * 102400u + h * 51200u + (10 + k) * 4096u);
      }
      pin();
      float c1[10];
      if constexpr (L == 0) {
        if (!p2ok) wait_flags(c, F_P2(160 + j * 20), 20, 9);
        if (d < 3) {
          gather_load(c1, c.rws, WS_CP1 + j * (20u * 384u) + d * 128u, 384u, lane);
          const float cs = gather_sum(c1);
          if (lane < 32) *(LAS float*)(c.lds + L_CS1 + (32 * d + lane) * 4) = cs;
        }
        pin();
      }
#pragma unroll
      for (int k = (L == 0 || !R1_EARLY) ? 0 : (R1_EARLY == 1 ? 30 : R1N); k < 30; ++k) r1[k] = ld16(c.rws, voff, WS_P1 + j * 122880u + k * 4096u);
      pin();
      const unsigned w2s = WS_P2 + cs8 * 245760u;
      if constexpr (L == 0) if (!p2ok) wait_flags(c, F_P2(480 + cs8 * 48), 48, 10);
#pragma unroll
      for (int k = 0; k < W2_EARLY; ++k)
#pragma unroll
        for (int h = 0; h < 2; ++h) r2[h][k] = ld16(c.rws, voff, w2s + h * 122880u + k * 4096u);
      pin();
      if (L == 1 || PZ_START != 2) poison_h(c, L);
      TS(11);
      bar_raw();
#pragma unroll
      for (int h = 0; h < 2; ++h) {
        lds16(lw + h * 10240, ro[h][0]); lds16(lw + h * 10240 + 4096, ro[h][1]);
        if (d < 2) lds16(lw + h * 10240 + 8192, ro[h][2]);
      }
#pragma unroll
      for (int k = 0; k < 7; ++k) lds16(lw + L_W1A + k * 4096, r1[k]);
      asm volatile("s_waitcnt vmcnt(0) lgkmcnt(0)" ::: "memory");
      TS(16);
      bar_raw();
      if constexpr (L == 0) { poison_x(c, WS_XI + XF_L, WS_STI + ST_L); poison_o(c, 1); poison_x(c, WS_X1 + XF_L, WS_ST1 + ST_L); }
      bar_raw();
#pragma unroll
      for (int k = 7; k < 27; ++k) lds16(lw + L_W1A + k * 4096, r1[k]);
#pragma unroll
      for (int k = 27; k < 30; ++k) lds16(lw + (k - 27) * 4096, r1[k]);
      asm volatile("s_waitcnt vmcnt(0) lgkmcnt(0)" ::: "memory");
      TS(23);
      bar_raw();
      bar_raw();
      pin();
#pragma unroll
      for (int k = W2_EARLY; k < 25; ++k) {
#pragma unroll
        for (int h = 0; h < 2; ++h) r2[h][k] = ld16(c.rws, voff, w2s + h * 122880u + k * 4096u);
      }
      pin();
      TS(24);
      bar_raw();
#pragma unroll
      for (int h = 0; h < 2; ++h) {
#pragma unroll
        for (int k = 0; k < 18; ++k) lds16(lw + h * 76800 + k * 4096, r2[h][k]);
        if (d < 3) lds16(lw + h * 76800 + 18 * 4096, r2[h][18]);
      }
      asm volatile("s_waitcnt lgkmcnt(0)" ::: "memory");
      TS(30);
      bar_raw();
#pragma unroll
      for (int k = 25; k < 30; ++k)
#pragma unroll
        for (int h = 0; h < 2; ++h) r2[h][k] = ld16(c.rws, voff, w2s + h * 122880u + k * 4096u);
      pin();
      u32x4 rq[38];
      const unsigned pqs = WS_PQ + j * 153600u;
      bar_raw();
#pragma unroll
      for (int h = 0; h < 2; ++h) {
        LAS char* q = lw + h * 76800 - 75 * 1024;
        if (d == 3) lds16(q + 18 * 4096, r2[h][18]);
#pragma unroll
        for (int k = 19; k < 25; ++k) lds16(q + k * 4096, r2[h][k]);
      }
      asm volatile("s_waitcnt lgkmcnt(0)" ::: "memory");
      bar_raw();
#pragma unroll
      for (int h = 0; h < 2; ++h) {
        LAS char* q = lw + h * 76800 - 75 * 1024;
#pragma unroll
        for (int k = 25; k < 30; ++k) lds16(q + k * 4096, r2[h][k]);
      }
      asm volatile("s_waitcnt lgkmcnt(0)" ::: "memory");
      if constexpr (L == 0) {
#pragma unroll
        for (int k = 0; k < RQ_A; ++k) rq[k] = ld16(c.rws, voff, pqs + k * 4096u);
      }
      pin();
      bar_raw();
      if constexpr (L == 0) {
#pragma unroll
        for (int k = RQ_A; k < RQ_A + RQ_B; ++k) rq[k] = ld16(c.rws, voff, pqs + k * 4096u);
      }
      pin();
      bar_raw();
      if constexpr (L == 0) {
#pragma unroll
        for (int k = RQ_A + RQ_B; k < 38; ++k) rq[k] = ld16(c.rws, voff, pqs + k * 4096u);
        pin();
        bar_raw();
#pragma unroll
        for (int k = 0; k < 32; ++k) lds16(lw + k * 4096, rq[k]);
#pragma unroll
        for (int k = 32; k < 37; ++k) lds16(lw + k * 4096, rq[k]);
        if (d < 2) lds16(lw + 37 * 4096, rq[37]);
        asm volatile("s_waitcnt lgkmcnt(0)" ::: "memory");
      }
      TS(39);
      TSFLUSH(L);
    };
    layer(IC<0>());
    layer(IC<1>());
    if (b == 0 && d == 0) {
      GAS u64* arr = (GAS u64*)((char*)p.ws + WS_ARR) + c.lane;
      unsigned spins = 0;
      while (!c.dead) {
        const u64 v0 = __hip_atomic_load(arr, __ATOMIC_RELAXED, __HIP_MEMORY_SCOPE_AGENT);
        const u64 v1 = __hip_atomic_load(arr + 64, __ATOMIC_RELAXED, __HIP_MEMORY_SCOPE_AGENT);
        const u64 v2 = __hip_atomic_load(arr + 128, __ATOMIC_RELAXED, __HIP_MEMORY_SCOPE_AGENT);
        const u64 v3 = __hip_atomic_load(arr + 192, __ATOMIC_RELAXED, __HIP_MEMORY_SCOPE_AGENT);
        if (__all(v0 == c.tag && v1 == c.tag && v2 == c.tag && v3 == c.tag)) break;
        if (++spins > 40000u) c.dead = true;
        __builtin_amdgcn_s_sleep(1);
      }
      if (c.lane == 0) __hip_atomic_store((GAS u64*)((char*)p.ws + WS_Z), zval + 1, __ATOMIC_RELAXED, __HIP_MEMORY_SCOPE_AGENT);
    }
  }
}

extern "C" void kernel_launch(void* const* d_in, const int* in_sizes, int n_in, void* d_out, int out_size, void* d_ws, size_t ws_size,
                              hipStream_t stream) {
  (void)in_sizes; (void)n_in; (void)out_size; (void)ws_size;
  Params p;
  p.x = d_in[0]; p.wqkv = d_in[1]; p.wo = d_in[2]; p.w1 = d_in[4]; p.b1 = d_in[5]; p.w2 = d_in[6]; p.b2 = d_in[7];
  p.out = d_out; p.ws = d_ws;
  (void)hipFuncSetAttribute((const void*)fused_kernel, hipFuncAttributeMaxDynamicSharedMemorySize, LDS_BYTES);
  fused_kernel<<<256, 512, LDS_BYTES, stream>>>(p);
}
```
